```python
import torch

def _softmax(x, axis=-1):
    return torch.softmax(x, dim=axis)

def _gelu(x):
    return torch.nn.functional.gelu(x, approximate='tanh')

def _silu(x):
    return torch.nn.functional.silu(x)

def _sigmoid(x):
    return torch.sigmoid(x)

def _layernorm(x, axis=-1, eps=1e-05):
    m = x.mean(dim=axis, keepdim=True)
    v = x.var(dim=axis, keepdim=True, unbiased=False)
    return (x - m) / torch.sqrt(v + eps)

def _scatter_add(tgt, idx, src, axis=0):
    return tgt.clone().index_add_(axis, idx, src)

def _segment_sum(src, seg, n):
    out = torch.zeros((n,) + src.shape[1:], dtype=src.dtype)
    return out.index_add_(0, seg, src)

def _pad(x, pw):
    flat = tuple((v for pair in reversed(pw) for v in pair))
    return torch.nn.functional.pad(x, flat)
kernel_name = 'adaLN_cond__sliding_block_cached__glu_gated_reglu__decode'

def setup_inputs():

    _w = lambda a: (lambda t: t.to(torch.bfloat16) if t.is_floating_point() else t)(torch.as_tensor(a))

    def _A_setup():
        B, Sq, D = (4, 1, 640)
        Hq, Hkv, Sk, Dh = (32, 32, 4096, 192)
        assert Hq % Hkv == 0, (Hq, Hkv)
        return {'x': _w((lambda *s: torch.randn(*s, dtype=torch.bfloat16) * 0.1)(B, Sq, D)), 'k_cache': _w((lambda *s: torch.randn(*s, dtype=torch.bfloat16) * 0.8)(B, Hkv, Sk, Dh)), 'v_cache': _w((lambda *s: torch.randn(*s, dtype=torch.bfloat16) * 3.2)(B, Hkv, Sk, Dh)), 'wq': _w((lambda *s: torch.randn(*s, dtype=torch.bfloat16) * 0.8)(D, Hq * Dh) * D ** (-0.5)), 'wo': _w((lambda *s: torch.randn(*s, dtype=torch.bfloat16) * 3.2)(Hq * Dh, D) * (Hq * Dh) ** (-0.5))}
    _A_in = _A_setup()

    def _B_setup():
        B, S, D, F = (4, 1, 640, 1536)
        return {'x': _w((lambda *s: torch.randn(*s, dtype=torch.bfloat16) * 0.1)(B, S, D)), 'W_gate': _w((lambda *s: torch.randn(*s, dtype=torch.bfloat16) * 3.2)(D, F) * (2.0 / D) ** 0.5), 'W_up': _w((lambda *s: torch.randn(*s, dtype=torch.bfloat16) * 3.2)(D, F) * (2.0 / D) ** 0.5), 'W_down': _w((lambda *s: torch.randn(*s, dtype=torch.bfloat16) * 3.2)(F, D) * (1.0 / F) ** 0.5)}
    _B_in = _B_setup()
    return {'A_x': _A_in['x'], 'A_k_cache': _A_in['k_cache'], 'A_v_cache': _A_in['v_cache'], 'A_wq': _A_in['wq'], 'A_wo': _A_in['wo'], 'B_x': _B_in['x'], 'B_W_gate': _B_in['W_gate'], 'B_W_up': _B_in['W_up'], 'B_W_down': _B_in['W_down'], 'c': _w((lambda *s: torch.randn(*s, dtype=torch.bfloat16) * 0.4)(4, 640) * 8.0), 'w_mod': _w((lambda *s: torch.randn(*s, dtype=torch.bfloat16) * 0.4)(640, 6 * 640) * 640 ** (-0.5) * 8.0)}

def reference(A_x, A_k_cache, A_v_cache, A_wq, A_wo, B_x, B_W_gate, B_W_up, B_W_down, c, w_mod):

    def _A_ref(x, k_cache, v_cache, wq, wo):
        B, Sq, D = x.shape
        Hq, Dh = (32, 192)
        Hkv, Sk = (k_cache.shape[1], k_cache.shape[2])
        W = min(4096, max(256, Sk // 4))
        rep = Hq // Hkv
        q = (lambda x, *d: x.permute(*d))((x @ wq).reshape(B, Sq, Hq, Dh), 0, 2, 1, 3)
        k = (lambda x, n, axis: torch.repeat_interleave(x, n, dim=axis))(k_cache[:, :, Sk - W:, :], rep, axis=1)
        v = (lambda x, n, axis: torch.repeat_interleave(x, n, dim=axis))(v_cache[:, :, Sk - W:, :], rep, axis=1)
        scores = torch.einsum('bhqd,bhkd->bhqk', q, k) * Dh ** (-0.5)
        p = _softmax(scores, axis=-1)
        o = torch.einsum('bhqk,bhkd->bhqd', p, v)
        return (lambda x, *d: x.permute(*d))(o, 0, 2, 1, 3).reshape(B, Sq, Hq * Dh) @ wo

    def _B_ref(x, W_gate, W_up, W_down):
        gate = torch.einsum('bsd,df->bsf', x, W_gate)
        up = torch.einsum('bsd,df->bsf', x, W_up)
        gate = gate * (lambda x, dt: x.to(dt))(gate > 0, gate.dtype)
        h = gate * up
        return torch.einsum('bsf,fd->bsd', h, W_down)
    x = A_x
    mod = (_silu(c) @ w_mod).reshape(c.shape[0], 6, 1, -1)
    sa, ca, ga, sb, cb, gb = (mod[:, 0].clone(), mod[:, 1].clone(), mod[:, 2].clone(), mod[:, 3].clone(), mod[:, 4].clone(), mod[:, 5].clone())
    h = _layernorm(x, axis=-1) * (1 + ca) + sa
    x = x + ga * _A_ref(h, A_k_cache, A_v_cache, A_wq, A_wo)
    h = _layernorm(x, axis=-1) * (1 + cb) + sb
    x = x + gb * _B_ref(h, B_W_gate, B_W_up, B_W_down)
    return x
```

```cpp
#include <hip/hip_runtime.h>
#include <hip/hip_bf16.h>
#include <stdint.h>

typedef unsigned short u16;
typedef unsigned u32x4 __attribute__((ext_vector_type(4)));
__device__ __forceinline__ float bf2f(u16 b) { return __builtin_bit_cast(float, (unsigned)b << 16); }
__device__ __forceinline__ float rbf(float f) {
  __bf16 h = (__bf16)f; return (float)h; }
__device__ __forceinline__ u16 f2bf(float f) { __bf16 h = (__bf16)f; return __builtin_bit_cast(u16, h); }

constexpr int B = 4, D = 640, H = 32, SK = 4096, DH = 192, W = 1024, F = 1536;

__device__ void ln_row(const float* xr, float* out, int tid, float* red) {
  float s = 0.f;
  for (int i = tid; i < D; i += 256) s += xr[i];
  for (int o = 32; o; o >>= 1) s += __shfl_xor(s, o);
  if ((tid & 63) == 0) red[tid >> 6] = s;
  __syncthreads();
  float mean = (red[0] + red[1] + red[2] + red[3]) * (1.f / D);
  __syncthreads();
  float v = 0.f;
  for (int i = tid; i < D; i += 256) { float d = xr[i] - mean; v += d * d; }
  for (int o = 32; o; o >>= 1) v += __shfl_xor(v, o);
  if ((tid & 63) == 0) red[tid >> 6] = v;
  __syncthreads();
  float var = (red[0] + red[1] + red[2] + red[3]) * (1.f / D);
  __syncthreads();
  float mb = rbf(mean), vb = rbf(var);
  float den = rbf(sqrtf(rbf(vb + 1e-5f)));
  for (int i = tid; i < D; i += 256) out[i] = rbf(rbf(xr[i] - mb) / den);
}

template <int K, int N>
__device__ void gemv64(const float* a_lds, const u16* Wt, int n0, float* red  , float* res  ) {
  int tid = threadIdx.x, ch = tid & 7, kg = tid >> 3;
  float acc[4][8] = {};
  for (int k = kg; k < K; k += 32) {
    u32x4 w = *(const u32x4*)(Wt + (size_t)k * N + n0 + ch * 8);
    float wf[8];
    for (int j = 0; j < 4; ++j) { wf[2*j] = __builtin_bit_cast(float, w[j] << 16); wf[2*j+1] = __builtin_bit_cast(float, w[j] & 0xffff0000u); }
    for (int b = 0; b < 4; ++b) { float av = a_lds[b * K + k]; for (int j = 0; j < 8; ++j) acc[b][j] = fmaf(av, wf[j], acc[b][j]); }
  }
  for (int b = 0; b < 4; ++b) for (int j = 0; j < 8; ++j) red[(kg * 4 + b) * 64 + ch * 8 + j] = acc[b][j];
  __syncthreads();
  { int b = tid >> 6, c = tid & 63; float s = 0.f; for (int g = 0; g < 32; ++g) s += red[(g * 4 + b) * 64 + c]; res[b * 64 + c] = s; }
  __syncthreads();
}

__global__ void __launch_bounds__(256) k_mod(const u16* c, const u16* wmod, float* mod) {
  __shared__ float a[4 * D]; __shared__ float red[32 * 4 * 64]; __shared__ float res[256];
  for (int i = threadIdx.x; i < 4 * D; i += 256) { float v = bf2f(c[i]); a[i] = rbf(v / (1.f + __expf(-v))); }
  __syncthreads();
  gemv64<D, 6 * D>(a, wmod, blockIdx.x * 64, red, res);
  int b = threadIdx.x >> 6, cc = threadIdx.x & 63;
  mod[b * 6 * D + blockIdx.x * 64 + cc] = rbf(res[threadIdx.x]);
}
__global__ void __launch_bounds__(256) k_q(const u16* x, const float* mod, const u16* wq, float* q) {
  __shared__ float a[4 * D]; __shared__ float xr[D]; __shared__ float red[32 * 4 * 64]; __shared__ float res[256];
  for (int b = 0; b < 4; ++b) {
    for (int i = threadIdx.x; i < D; i += 256) xr[i] = bf2f(x[b * D + i]);
    __syncthreads();
    ln_row(xr, a + b * D, threadIdx.x, red);
    for (int i = threadIdx.x; i < D; i += 256) a[b * D + i] = rbf(rbf(a[b * D + i] * rbf(1.f + mod[b * 6 * D + D + i])) + mod[b * 6 * D + i]);
    __syncthreads();
  }
  gemv64<D, H * DH>(a, wq, blockIdx.x * 64, red, res);
  int b = threadIdx.x >> 6, cc = threadIdx.x & 63;
  q[b * H * DH + blockIdx.x * 64 + cc] = rbf(res[threadIdx.x]);
}
__global__ void __launch_bounds__(256) k_attn(const float* q, const u16* kc, const u16* vc, float* o) {
  __shared__ float qs[DH]; __shared__ float sc[W]; __shared__ float red[8]; __shared__ float part[8][DH];
  int bh = blockIdx.x, tid = threadIdx.x;
  if (tid < DH) qs[tid] = q[bh * DH + tid];
  __syncthreads();
  const u16* kb = kc + ((size_t)bh * SK + (SK - W)) * DH;
  const u16* vb = vc + ((size_t)bh * SK + (SK - W)) * DH;
  int sub = tid & 7, kr = tid >> 3;
  for (int k0 = 0; k0 < W; k0 += 32) {
    int key = k0 + kr; float s = 0.f;
    for (int t = 0; t < 3; ++t) {
      u32x4 w = *(const u32x4*)(kb + (size_t)key * DH + (t * 8 + sub) * 8);
      for (int j = 0; j < 4; ++j) { s = fmaf(__builtin_bit_cast(float, w[j] << 16), qs[(t * 8 + sub) * 8 + 2 * j], s); s = fmaf(__builtin_bit_cast(float, w[j] & 0xffff0000u), qs[(t * 8 + sub) * 8 + 2 * j + 1], s); }
    }
    s += __shfl_xor(s, 1); s += __shfl_xor(s, 2); s += __shfl_xor(s, 4);
    if (sub == 0) sc[key] = rbf(rbf(s) * 0.07216878364870322f);
  }
  __syncthreads();
  float m = -INFINITY;
  for (int i = tid; i < W; i += 256) m = fmaxf(m, sc[i]);
  for (int off = 32; off; off >>= 1) m = fmaxf(m, __shfl_xor(m, off));
  if ((tid & 63) == 0) red[tid >> 6] = m;
  __syncthreads();
  m = fmaxf(fmaxf(red[0], red[1]), fmaxf(red[2], red[3]));
  float l = 0.f;
  for (int i = tid; i < W; i += 256) { float p = __expf(sc[i] - m); sc[i] = p; l += p; }
  for (int off = 32; off; off >>= 1) l += __shfl_xor(l, off);
  if ((tid & 63) == 0) red[4 + (tid >> 6)] = l;
  __syncthreads();
  l = red[4] + red[5] + red[6] + red[7];
  float inv = 1.f / l;
  if (tid < 192) {
    int ch = tid % 24, g = tid / 24; float acc[8] = {};
    for (int key = g; key < W; key += 8) {
      u32x4 w = *(const u32x4*)(vb + (size_t)key * DH + ch * 8); float p = rbf(sc[key] * inv);
      for (int j = 0; j < 4; ++j) { acc[2*j] = fmaf(p, __builtin_bit_cast(float, w[j] << 16), acc[2*j]); acc[2*j+1] = fmaf(p, __builtin_bit_cast(float, w[j] & 0xffff0000u), acc[2*j+1]); }
    }
    for (int j = 0; j < 8; ++j) part[g][ch * 8 + j] = acc[j];
  }
  __syncthreads();
  if (tid < DH) { float s = 0.f; for (int g = 0; g < 8; ++g) s += part[g][tid]; o[bh * DH + tid] = rbf(s); }
}
__global__ void __launch_bounds__(256) k_wo(const float* o, const u16* wo, float* part) {
  __shared__ float a[4 * 256]; __shared__ float red[32 * 4 * 64]; __shared__ float res[256];
  int kc = blockIdx.y;
  for (int i = threadIdx.x; i < 4 * 256; i += 256) a[i] = o[(i >> 8) * H * DH + kc * 256 + (i & 255)];
  __syncthreads();
  gemv64<256, D>(a, wo + (size_t)kc * 256 * D, blockIdx.x * 64, red, res);
  int b = threadIdx.x >> 6, cc = threadIdx.x & 63;
  part[(kc * 4 + b) * D + blockIdx.x * 64 + cc] = res[threadIdx.x];
}
__global__ void __launch_bounds__(256) k_gu(const u16* x, const float* mod, const float* part, const u16* wg, const u16* wu, float* x1o, float* hh) {
  __shared__ float a[4 * D]; __shared__ float xr[D]; __shared__ float red[32 * 4 * 64]; __shared__ float res[256]; __shared__ float res2[256];
  for (int b = 0; b < 4; ++b) {
    for (int i = threadIdx.x; i < D; i += 256) {
      float s = 0.f; for (int k = 0; k < 24; ++k) s += part[(k * 4 + b) * D + i];
      float v = rbf(bf2f(x[b * D + i]) + rbf(mod[b * 6 * D + 2 * D + i] * rbf(s)));
      xr[i] = v; if (blockIdx.x == 0) x1o[b * D + i] = v;
    }
    __syncthreads();
    ln_row(xr, a + b * D, threadIdx.x, red);
    for (int i = threadIdx.x; i < D; i += 256) a[b * D + i] = rbf(rbf(a[b * D + i] * rbf(1.f + mod[b * 6 * D + 4 * D + i])) + mod[b * 6 * D + 3 * D + i]);
    __syncthreads();
  }
  gemv64<D, F>(a, wg, blockIdx.x * 64, red, res);
  gemv64<D, F>(a, wu, blockIdx.x * 64, red, res2);
  int b = threadIdx.x >> 6, cc = threadIdx.x & 63;
  float g = rbf(res[threadIdx.x]), u = rbf(res2[threadIdx.x]);
  g = g * (g > 0.f ? 1.f : 0.f);
  hh[b * F + blockIdx.x * 64 + cc] = rbf(g * u);
}
__global__ void __launch_bounds__(256) k_down(const float* hh, const u16* wd, const float* mod, const float* x1, u16* out) {
  __shared__ float a[4 * F]; __shared__ float red[32 * 4 * 64]; __shared__ float res[256];
  for (int i = threadIdx.x; i < 4 * F; i += 256) a[i] = hh[i];
  __syncthreads();
  gemv64<F, D>(a, wd, blockIdx.x * 64, red, res);
  int b = threadIdx.x >> 6, cc = threadIdx.x & 63, n = blockIdx.x * 64 + cc;
  out[b * D + n] = f2bf(x1[b * D + n] + rbf(mod[b * 6 * D + 5 * D + n] * rbf(res[threadIdx.x])));
}

extern "C" void kernel_launch(void* const* d_in, const int* in_sizes, int n_in, void* d_out, int out_size, void* d_ws, size_t ws_size, hipStream_t stream) {
  const u16* x = (const u16*)d_in[0]; const u16* kc = (const u16*)d_in[1]; const u16* vc = (const u16*)d_in[2];
  const u16* wq = (const u16*)d_in[3]; const u16* wo = (const u16*)d_in[4];
  const u16* wg = (const u16*)d_in[6]; const u16* wu = (const u16*)d_in[7]; const u16* wd = (const u16*)d_in[8];
  const u16* c = (const u16*)d_in[9]; const u16* wmod = (const u16*)d_in[10];
  float* ws = (float*)d_ws;
  float* mod = ws; float* q = mod + 4 * 6 * D; float* o = q + 4 * H * DH; float* part = o + 4 * H * DH; float* x1 = part + 24 * 4 * D; float* hh = x1 + 4 * D;
  k_mod<<<60, 256, 0, stream>>>(c, wmod, mod);
  k_q<<<96, 256, 0, stream>>>(x, mod, wq, q);
  k_attn<<<128, 256, 0, stream>>>(q, kc, vc, o);
  k_wo<<<dim3(10, 24), 256, 0, stream>>>(o, wo, part);
  k_gu<<<24, 256, 0, stream>>>(x, mod, part, wg, wu, x1, hh);
  k_down<<<10, 256, 0, stream>>>(hh, wd, mod, x1, (u16*)d_out);
}
```

```cpp
#include <hip/hip_runtime.h>
#include <hip/hip_bf16.h>
#include <stdint.h>

#define W_WIN 5
#ifndef NV0
#define NV0 9
#endif
#define STAMPS 0

typedef unsigned short u16;
typedef unsigned u32x4 __attribute__((ext_vector_type(4)));
typedef unsigned u32x2 __attribute__((ext_vector_type(2)));
typedef float f32x4 __attribute__((ext_vector_type(4)));
typedef short s16x4 __attribute__((ext_vector_type(4)));
typedef short s16x8 __attribute__((ext_vector_type(8)));
typedef __bf16 bf16x2 __attribute__((ext_vector_type(2)));
typedef __bf16 bf16x8 __attribute__((ext_vector_type(8)));
#define LAS __attribute__((address_space(3)))
#define GAS __attribute__((address_space(1)))
typedef __amdgpu_buffer_rsrc_t rsrc_t;

constexpr int D = 640, NH = 32, DH = 192, SK = 4096, WIN = 1024, FF = 1536;
constexpr int AUX_NT = 2, AUX_SC1 = 16;
constexpr unsigned SPIN_CAP = 1u << 16;

constexpr int WS_EPOCH = 0;
constexpr int WS_HG = 1024;
constexpr int WS_XG = WS_HG + 4 * 320 * 8;
constexpr int WS_MG = WS_XG + 4 * 320 * 8;
constexpr int WS_ML = WS_MG + 4 * 1280 * 8;
constexpr int WS_OG = WS_ML + 256 * 16;
constexpr int WS_QG = WS_OG + 256 * 96 * 16;
constexpr int WS_YG = WS_QG + 32 * 4 * 384 * 16;
constexpr int WS_PG = WS_YG + 4 * 40 * 32 * 128;
constexpr int WS_END = WS_PG + 4 * 40 * 96 * 128;
constexpr int WS_STAMP = 8 << 20;

constexpr int L_WQ = 0;
constexpr int L_WO = L_WQ + 160 * 192 + 20 * 32;
constexpr int L_RL = L_WO + 192 * 160 + 24 * 128;
constexpr int L_WD = L_RL + 80 * 544;
constexpr int L_A = L_WD + 16 * 1312;
constexpr int L_HA = L_A + 4 * 1344;
constexpr int L_OA = L_HA + 4 * 320;
constexpr int L_Q = L_OA + 4 * 448;
constexpr int L_OP = L_Q + 384;
constexpr int L_RED = L_OP + 8 * 1552;
constexpr int L_MISC = L_RED + 8 * 256;
constexpr int L_BYTES = L_MISC + 256;
static_assert(L_BYTES <= 160 * 1024 && L_BYTES > 80 * 1024, "one workgroup per CU");

__device__ __forceinline__ float bflo(unsigned u) { return __builtin_bit_cast(float, u << 16); }
__device__ __forceinline__ float bfhi(unsigned u) { return __builtin_bit_cast(float, u & 0xffff0000u); }
__device__ __forceinline__ float rbf(float f) { return (float)(__bf16)f; }
__device__ __forceinline__ unsigned pack2(float lo, float hi) { bf16x2 v = {(__bf16)lo, (__bf16)hi}; return __builtin_bit_cast(unsigned, v); }
__device__ __forceinline__ unsigned f2u(float f) { return __builtin_bit_cast(unsigned, f); }
__device__ __forceinline__ float u2f(unsigned u) { return __builtin_bit_cast(float, u); }
__device__ __forceinline__ float dot2(unsigned a, unsigned b, float c) {
  return __builtin_amdgcn_fdot2_f32_bf16(__builtin_bit_cast(bf16x2, a), __builtin_bit_cast(bf16x2, b), c, false);
}
template <int CTRL> __device__ __forceinline__ float dpp(float x) { return u2f((unsigned)__builtin_amdgcn_mov_dpp((int)f2u(x), CTRL, 0xf, 0xf, true)); }
template <int CTRL> __device__ __forceinline__ unsigned dppu(unsigned x) { return (unsigned)__builtin_amdgcn_mov_dpp((int)x, CTRL, 0xf, 0xf, true); }
constexpr int XOR1 = 0xB1, XOR2 = 0x4E, XOR7 = 0x141, XOR8 = 0x128, XOR15 = 0x140;
__device__ __forceinline__ float row_sum(float v) { v += dpp<XOR1>(v); v += dpp<XOR2>(v); v += dpp<XOR7>(v); v += dpp<XOR8>(v); return v; }
__device__ __forceinline__ float row_max(float v) { v = fmaxf(v, dpp<XOR1>(v)); v = fmaxf(v, dpp<XOR2>(v)); v = fmaxf(v, dpp<XOR7>(v)); v = fmaxf(v, dpp<XOR8>(v)); return v; }
__device__ __forceinline__ float xrow_sum(float x) {
  auto s = __builtin_amdgcn_permlane16_swap(f2u(x), f2u(x), false, false);
  x = u2f(s[0]) + u2f(s[1]);
  auto t = __builtin_amdgcn_permlane32_swap(f2u(x), f2u(x), false, false);
  return u2f(t[0]) + u2f(t[1]);
}
__device__ __forceinline__ float xrow_max(float x) {
  auto s = __builtin_amdgcn_permlane16_swap(f2u(x), f2u(x), false, false);
  x = fmaxf(u2f(s[0]), u2f(s[1]));
  auto t = __builtin_amdgcn_permlane32_swap(f2u(x), f2u(x), false, false);
  return fmaxf(u2f(t[0]), u2f(t[1]));
}
__device__ __forceinline__ float wave_sum(float v) { return xrow_sum(row_sum(v)); }
__device__ __forceinline__ rsrc_t mkrs(const void* p, unsigned bytes) { return __builtin_amdgcn_make_buffer_rsrc((void*)p, 0, bytes, 0x00020000); }
__device__ __forceinline__ u32x4 ldnt(rsrc_t rs, int voff, int soff) { return __builtin_amdgcn_raw_buffer_load_b128(rs, voff, soff, AUX_NT); }
__device__ __forceinline__ u32x4 ldsc(rsrc_t rs, int voff) { return __builtin_amdgcn_raw_buffer_load_b128(rs, voff, 0, AUX_SC1); }
__device__ __forceinline__ u32x2 ldsc8(rsrc_t rs, int voff) { return __builtin_amdgcn_raw_buffer_load_b64(rs, voff, 0, AUX_SC1); }
__device__ __forceinline__ void stsc(rsrc_t rs, int voff, u32x4 v) { __builtin_amdgcn_raw_buffer_store_b128(v, rs, voff, 0, AUX_SC1); }
__device__ __forceinline__ void stsc8(rsrc_t rs, int voff, u32x2 v) { __builtin_amdgcn_raw_buffer_store_b64(v, rs, voff, 0, AUX_SC1); }
template <int N> __device__ __forceinline__ void waitvm() { asm volatile("s_waitcnt vmcnt(%0)" ::"i"(N) : "memory"); }
template <int N> __device__ __forceinline__ void pace(int w) {
  asm volatile("s_cmp_eq_u32 %0, 0\n\ts_cbranch_scc1 1f\n\ts_waitcnt vmcnt(%1)\n1:" ::"s"(w), "i"(N) : "scc", "memory");
}
#define SCHED() __builtin_amdgcn_sched_barrier(0)
#define MEMBAR() asm volatile("" ::: "memory")
__device__ __forceinline__ void wg_barrier() { asm volatile("s_waitcnt lgkmcnt(0)\n\ts_barrier" ::: "memory"); }
__device__ __forceinline__ f32x4 mfma32(u32x4 a, u32x4 b, f32x4 c) {
  return __builtin_amdgcn_mfma_f32_16x16x32_bf16(__builtin_bit_cast(bf16x8, a), __builtin_bit_cast(bf16x8, b), c, 0, 0, 0);
}
__device__ __forceinline__ u32x2 trread(LAS unsigned char* p) { return __builtin_bit_cast(u32x2, __builtin_amdgcn_ds_read_tr16_b64_v4i16((LAS s16x4*)p)); }
__device__ __forceinline__ u32x4 trfrag(LAS unsigned char* p, int step) {
  const u32x2 lo = trread(p), hi = trread(p + step);
  return u32x4{lo.x, lo.y, hi.x, hi.y};
}
__device__ __forceinline__ float sel4(f32x4 v, int g) { const float a = (g & 1) ? v[1] : v[0], b = (g & 1) ? v[3] : v[2]; return (g & 2) ? b : a; }
__device__ __forceinline__ unsigned tag2(u32x4 v, unsigned tag) { return (v.y ^ tag) | (v.w ^ tag); }
__device__ __forceinline__ bool all0(unsigned bad) { return __builtin_amdgcn_ballot_w64(bad != 0u) == 0ull; }

#define STAMP(n) do {} while (0)

__global__ void __launch_bounds__(512, 2)
fused_block(const u16* __restrict__ xin, const u16* __restrict__ kc, const u16* __restrict__ vc, const u16* __restrict__ wq,
            const u16* __restrict__ wo, const u16* __restrict__ wg, const u16* __restrict__ wu, const u16* __restrict__ wd,
            const u16* __restrict__ cin, const u16* __restrict__ wmod, u16* __restrict__ out, unsigned char* ws) {
  __shared__ __attribute__((aligned(16))) unsigned char smem[L_BYTES];
  LAS unsigned char* L = (LAS unsigned char*)smem;
  volatile LAS unsigned* MISC = (volatile LAS unsigned*)(L + L_MISC);
  const int tid = threadIdx.x, lane = tid & 63, w = __builtin_amdgcn_readfirstlane(tid >> 6), g = lane >> 4, c = lane & 15;
  const int bid = blockIdx.x, x = bid & 7, slot = bid >> 3, h = 4 * x + (slot >> 3), j8 = slot & 7, b = j8 >> 1, s = j8 & 1;
  const bool isMod = slot < 15, isHU = isMod && x < 4 && slot < 10, isRed = slot < 20, isMlp = slot >= 20;
  const int hu = 10 * x + slot;
  const int ru = x < 4 ? 5 * x + (slot - 10) : 20 + 15 * (x - 4) + slot;
  const int rid = x * 20 + slot, rb = rid / 40, rt = rid % 40;
  const int mid = x * 12 + (slot - 20);
  STAMP(0);

  const rsrc_t rws = mkrs(ws, WS_END);
  const size_t kvbase = ((size_t)(b * NH + h) * SK + (SK - WIN) + s * 512 + w * 64) * DH;
  const rsrc_t rk = mkrs(kc + kvbase, 64 * 384), rv = mkrs(vc + kvbase, 64 * 384);
  const int kvo = g * 768 + c * 16;
  u32x4 K[24], V[24];
#define LDK(n) K[n] = ldnt(rk, kvo + ((n) % 3) * 256, ((n) / 3) * 3072)
#define LDV(n) V[n] = ldnt(rv, kvo + ((n) % 3) * 256, ((n) / 3) * 3072)

  u32x4 SR[8], SQ[4], SO[4];
  if (isMod) {
    const rsrc_t rm = mkrs(wmod, D * 6 * D * 2);
    const int c0 = isHU ? 16 * hu : 1280 + 32 * ru, c1 = isHU ? 640 + 16 * hu : 1296 + 32 * ru;
    const int ch = tid & 3, vo = (tid >> 2) * 7680 + ((ch & 2) ? c1 : c0) * 2 + (ch & 1) * 16;
#pragma unroll
    for (int k = 0; k < 5; ++k) SR[k] = ldnt(rm, vo, k * 128 * 7680);
  } else if (isMlp) {
    const rsrc_t rg = mkrs(w < 4 ? wg : wu, D * FF * 2), rd = mkrs(wd + (size_t)mid * 16 * D, 16 * D * 2);
    const int t2 = tid & 255, vo = (t2 >> 1) * 3072 + mid * 32 + (t2 & 1) * 16;
#pragma unroll
    for (int k = 0; k < 5; ++k) SR[k] = ldnt(rg, vo, k * 128 * 3072);
#pragma unroll
    for (int k = 0; k < 3; ++k) SR[5 + k] = ldnt(rd, tid * 16 + k * 8192, 0);
  }
  {
    const rsrc_t rq = mkrs(wq + (size_t)(160 * b) * (NH * DH) + DH * h + 96 * s, 160 * NH * DH * 2 - (DH * h + 96 * s) * 2);
    const rsrc_t ro = mkrs(wo + (size_t)(DH * h) * D + 80 * j8, DH * D * 2 - 80 * j8 * 2);
#pragma unroll
    for (int k = 0; k < 4; ++k) { const int u = tid + 512 * k, row = u / 12, ch = u % 12; SQ[k] = ldnt(rq, row * 12288 + ch * 16, 0); }
#pragma unroll
    for (int k = 0; k < 4; ++k) { const int u = tid + 512 * k, row = u / 10, ch = u % 10; SO[k] = ldnt(ro, row * 1280 + ch * 16, 0); }
  }
  SCHED();
  unsigned epoch = 0;
  if (w == 0) epoch = __hip_atomic_load((GAS unsigned*)(ws + WS_EPOCH), __ATOMIC_RELAXED, __HIP_MEMORY_SCOPE_AGENT);
  u32x4 cv = {0, 0, 0, 0};
  unsigned xr[5] = {0, 0, 0, 0, 0};
  unsigned xh = 0;
  if (isMod && tid < 320) cv = *(const u32x4*)(cin + tid * 8);
  if (isHU) {
    if (w >= 4) {
#pragma unroll
      for (int k = 0; k < 5; ++k) xr[k] = ((const unsigned*)xin)[(w - 4) * 320 + lane + 64 * k];
    }
    if (w == 0) xh = ((const unsigned*)xin)[g * 320 + 8 * hu + (c >> 1)];
  }
  unsigned xres = 0;
  if (isRed && tid < 8) xres = ((const unsigned*)xin)[rb * 320 + rt * 8 + tid];
  SCHED();
  auto prologue_finish = [&]() {
    if (tid == 0) { MISC[0] = (epoch + 1u) * 0x9E3779B1u; MISC[1] = 0u; MISC[2] = 0u; }
    if (isMod && tid < 320) {
      u32x4 o;
#pragma unroll
      for (int k = 0; k < 4; ++k) { const float a = bflo(cv[k]), bb = bfhi(cv[k]); o[k] = pack2(a / (1.f + __expf(-a)), bb / (1.f + __expf(-bb))); }
      *(LAS u32x4*)(L + L_A + (tid / 80) * 1344 + (tid % 80) * 16) = o;
    }
    if (isHU && w >= 4) {
      float sm = 0.f;
#pragma unroll
      for (int k = 0; k < 5; ++k) sm += bflo(xr[k]) + bfhi(xr[k]);
      const float mean = wave_sum(sm) * (1.f / D);
      float sv = 0.f;
#pragma unroll
      for (int k = 0; k < 5; ++k) { const float a = bflo(xr[k]) - mean, bb = bfhi(xr[k]) - mean; sv += a * a + bb * bb; }
      const float var = wave_sum(sv) * (1.f / D);
      if (lane == 0) { MISC[4 + 2 * (w - 4)] = f2u(rbf(mean)); MISC[5 + 2 * (w - 4)] = f2u(rbf(sqrtf(rbf(rbf(var) + 1e-5f)))); }
    }
    if (isMod) {
#pragma unroll
      for (int k = 0; k < 5; ++k) { const int row = (tid >> 2) + 128 * k; *(LAS u32x4*)(L + L_RL + row * 64 + (row >> 3) * 32 + (tid & 3) * 16) = SR[k]; }
    } else if (isMlp) {
      const int t2 = tid & 255;
#pragma unroll
      for (int k = 0; k < 5; ++k) { const int row = (t2 >> 1) + 128 * k; *(LAS u32x4*)(L + L_RL + row * 64 + (row >> 3) * 32 + (w >> 2) * 32 + (t2 & 1) * 16) = SR[k]; }
#pragma unroll
      for (int k = 0; k < 3; ++k) { const int u = tid + 512 * k; if (u < 1280) *(LAS u32x4*)(L + L_WD + (u / 80) * 1312 + (u % 80) * 16) = SR[5 + k]; }
    }
#pragma unroll
    for (int k = 0; k < 4; ++k) { const int u = tid + 512 * k, row = u / 12, ch = u % 12; if (u < 1920) *(LAS u32x4*)(L + L_WQ + row * 192 + (row >> 3) * 32 + ch * 16) = SQ[k]; }
#pragma unroll
    for (int k = 0; k < 4; ++k) { const int u = tid + 512 * k, row = u / 10, ch = u % 10; if (u < 1920) *(LAS u32x4*)(L + L_WO + row * 160 + (row >> 3) * 128 + ch * 16) = SO[k]; }
  };
  const int trq = (c >> 2), trp = (c & 3);

#pragma unroll
  for (int n = 0; n < W_WIN; ++n) LDK(n);
  SCHED();
  prologue_finish();
  wg_barrier();
  STAMP(1);
  const unsigned tag = MISC[0];
#pragma unroll
  for (int n = W_WIN; n < 24; ++n) { pace<W_WIN - 1>(w); LDK(n); SCHED(); }
  if (w == 0) {
    if (isMod) {
      f32x4 a0 = {0, 0, 0, 0}, a1 = {0, 0, 0, 0};
      LAS unsigned char* pa = L + L_A + (c & 3) * 1344 + g * 16;
      LAS unsigned char* pb = L + L_RL + g * 544 + trq * 64 + trp * 8;
#pragma unroll
      for (int ks = 0; ks < 20; ++ks) {
        const u32x4 af = *(LAS u32x4*)(pa + ks * 64);
        a0 = mfma32(af, trfrag(pb + ks * 2176, 256), a0);
        a1 = mfma32(af, trfrag(pb + ks * 2176 + 32, 256), a1);
      }
      const float v0 = rbf(sel4(a0, g)), v1 = rbf(sel4(a1, g));
      if (isHU) {
        const float mb = u2f(MISC[4 + 2 * g]), den = u2f(MISC[5 + 2 * g]);
        const float hv = rbf(rbf(rbf(rbf(((c & 1) ? bfhi(xh) : bflo(xh)) - mb) / den) * rbf(1.f + v1)) + v0);
        const float hn = dpp<XOR1>(hv);
        if ((c & 1) == 0) stsc8(rws, WS_HG + (g * 320 + 8 * hu + (c >> 1)) * 8, u32x2{pack2(hv, hn), tag});
      } else {
        const float n0 = dpp<XOR1>(v0), n1 = dpp<XOR1>(v1);
        const unsigned pv = (c & 1) ? pack2(n1, v1) : pack2(v0, n0);
        stsc8(rws, WS_MG + (g * 1280 + 16 * ru + (c & 1) * 8 + (c >> 1)) * 8, u32x2{pv, tag});
      }
    }
    STAMP(2);
    {
      u32x4 t[3];
      int off[3];
#pragma unroll
      for (int k = 0; k < 3; ++k) { const int n = min(lane + 64 * k, 159); off[k] = WS_HG + (n / 40) * 2560 + b * 640 + (n % 40) * 16; }
#pragma unroll 1
      for (unsigned spins = 0; spins < SPIN_CAP; ++spins) {
        MEMBAR();
#pragma unroll
        for (int k = 0; k < 3; ++k) t[k] = ldsc(rws, off[k]);
        if (all0(tag2(t[0], tag) | tag2(t[1], tag) | tag2(t[2], tag))) break;
        __builtin_amdgcn_s_sleep(4);
      }
#pragma unroll
      for (int k = 0; k < 3; ++k) { const int n = min(lane + 64 * k, 159); *(LAS u32x2*)(L + L_HA + (n / 40) * 320 + (n % 40) * 8) = u32x2{t[k].x, t[k].z}; }
    }
    STAMP(3);
    {
      f32x4 acc[6];
#pragma unroll
      for (int t = 0; t < 6; ++t) acc[t] = f32x4{0, 0, 0, 0};
      LAS unsigned char* pa = L + L_HA + (c & 3) * 320 + g * 16;
      LAS unsigned char* pb = L + L_WQ + g * 1568 + trq * 192 + trp * 8;
#pragma unroll
      for (int ks = 0; ks < 5; ++ks) {
        const u32x4 af = *(LAS u32x4*)(pa + ks * 64);
#pragma unroll
        for (int t = 0; t < 6; ++t) acc[t] = mfma32(af, trfrag(pb + ks * 6272 + t * 32, 768), acc[t]);
      }
#pragma unroll
      for (int tp = 0; tp < 3; ++tp)
        stsc(rws, WS_QG + ((((h * 4 + g) * 4 + b) * 2 + s) * 48 + tp * 16 + c) * 16, u32x4{f2u(sel4(acc[2 * tp], g)), tag, f2u(sel4(acc[2 * tp + 1], g)), tag});
    }
    STAMP(4);
    {
      u32x4 t[6];
      const int qb = WS_QG + (h * 4 + b) * 384 * 16;
      const int o1 = qb + lane * 16, o2 = qb + ((lane >> 5) * 192 + 64 + (lane & 31)) * 16;
#pragma unroll 1
      for (unsigned spins = 0; spins < SPIN_CAP; ++spins) {
        MEMBAR();
#pragma unroll
        for (int k = 0; k < 4; ++k) t[k] = ldsc(rws, o1 + k * 1536);
#pragma unroll
        for (int k = 0; k < 2; ++k) t[4 + k] = ldsc(rws, o2 + k * 1536);
        unsigned bad = 0;
#pragma unroll
        for (int k = 0; k < 6; ++k) bad |= tag2(t[k], tag);
        if (all0(bad)) break;
        __builtin_amdgcn_s_sleep(4);
      }
      const float qa = (u2f(t[0].x) + u2f(t[1].x)) + (u2f(t[2].x) + u2f(t[3].x)), qb2 = (u2f(t[0].z) + u2f(t[1].z)) + (u2f(t[2].z) + u2f(t[3].z));
      float qc = u2f(t[4].x) + u2f(t[5].x), qd = u2f(t[4].z) + u2f(t[5].z);
      { auto r = __builtin_amdgcn_permlane32_swap(f2u(qc), f2u(qc), false, false); qc = u2f(r[0]) + u2f(r[1]); }
      { auto r = __builtin_amdgcn_permlane32_swap(f2u(qd), f2u(qd), false, false); qd = u2f(r[0]) + u2f(r[1]); }
      LAS u16* Q = (LAS u16*)(L + L_Q);
      const int d1 = lane < 48 ? 32 * g + c : 96 + c;
      Q[d1] = (u16)pack2(qa, 0.f); Q[d1 + 16] = (u16)pack2(qb2, 0.f);
      const int d2 = 128 + 32 * (g & 1) + c;
      if (lane < 32) { Q[d2] = (u16)pack2(qc, 0.f); Q[d2 + 16] = (u16)pack2(qd, 0.f); }
      asm volatile("s_waitcnt lgkmcnt(0)" ::: "memory");
      MISC[1] = 1u;
    }
  }
  SCHED();
#pragma unroll
  for (int n = 0; n < NV0; ++n) { pace<W_WIN - 1>(w); LDV(n); SCHED(); }
  STAMP(5);
#pragma unroll 1
  for (unsigned spins = 0; MISC[1] == 0u && spins < (SPIN_CAP << 4); ++spins) __builtin_amdgcn_s_sleep(2);
  STAMP(13);
  SCHED();
#pragma unroll
  for (int n = NV0; n < 24; ++n) LDV(n);
  SCHED();

  unsigned pl;
  {
    LAS unsigned char* pq = L + L_Q;
    const u32x4 q0 = *(LAS u32x4*)(pq + c * 16), q1 = *(LAS u32x4*)(pq + ((16 + c) % 24) * 16), q2 = *(LAS u32x4*)(pq + (8 + c) * 16);
    float v[16];
#pragma unroll
    for (int a = 0; a < 8; ++a) {
      float d1 = 0.f;
#pragma unroll
      for (int k = 0; k < 4; ++k) d1 = dot2(K[3 * a + 1][k], q1[k], d1);
      float e = c < 8 ? d1 : 0.f, o = c < 8 ? 0.f : d1;
#pragma unroll
      for (int k = 0; k < 4; ++k) { e = dot2(K[3 * a][k], q0[k], e); o = dot2(K[3 * a + 2][k], q2[k], o); }
      v[2 * a] = e; v[2 * a + 1] = o;
    }
#define TR_STEP(HALF, CTRL) _Pragma("unroll") for (int k = 0; k < (HALF); ++k) { \
      const float t1 = v[k] + dpp<CTRL>(v[k]), t2 = v[k + (HALF)] + dpp<CTRL>(v[k + (HALF)]); v[k] = (c & (HALF)) ? t2 : t1; }
    TR_STEP(8, XOR8) TR_STEP(4, XOR7) TR_STEP(2, XOR2) TR_STEP(1, XOR1)
#undef TR_STEP
    const float sc = rbf(rbf(v[0]) * 0.07216878364870322f);
    const float mw = xrow_max(row_max(sc));
    const float p = __expf(sc - mw);
    const float lw = wave_sum(p);
    pl = pack2(p, 0.f);
    if (lane == 0) { MISC[16 + w] = f2u(mw); MISC[24 + w] = f2u(lw); }
    asm volatile("s_waitcnt lgkmcnt(0)" ::: "memory");
    unsigned old = 0;
    if (lane == 0) old = __hip_atomic_fetch_add((LAS unsigned*)(L + L_MISC + 8), 1u, __ATOMIC_RELAXED, __HIP_MEMORY_SCOPE_WORKGROUP);
    if (__builtin_amdgcn_readfirstlane(old) == 7u) {
      const float m = u2f(MISC[16 + (c & 7)]), l = u2f(MISC[24 + (c & 7)]);
      const float M = row_max(m);
      const float Ls = row_sum(l * __expf(m - M)) * 0.5f;
      if (lane == 0) stsc(rws, WS_ML + (h * 8 + j8) * 16, u32x4{f2u(M), tag, f2u(Ls), tag});
    }
  }
  STAMP(6);
  SCHED();
  u32x4 BO[6];
  if (w < 5) {
    LAS unsigned char* pb = L + L_WO + g * 1408 + trq * 160 + trp * 8 + w * 32;
#pragma unroll
    for (int ks = 0; ks < 6; ++ks) BO[ks] = trfrag(pb + ks * 5632, 640);
  }
  float acc[24];
#pragma unroll
  for (int k = 0; k < 24; ++k) acc[k] = 0.f;
  const unsigned ph = pl << 16;
  float fsc = 0.f;
#define PV(a) { \
    const unsigned el = dppu<0x150 + 2 * (a)>(pl), ol = dppu<0x151 + 2 * (a)>(pl), eh = dppu<0x150 + 2 * (a)>(ph), oh = dppu<0x151 + 2 * (a)>(ph); \
    const unsigned ml = c < 8 ? el : ol, mh = c < 8 ? eh : oh; \
    _Pragma("unroll") for (int k = 0; k < 4; ++k) { \
      acc[2 * k] = dot2(V[3 * (a)][k], el, acc[2 * k]); acc[2 * k + 1] = dot2(V[3 * (a)][k], eh, acc[2 * k + 1]); \
      acc[8 + 2 * k] = dot2(V[3 * (a) + 1][k], ml, acc[8 + 2 * k]); acc[9 + 2 * k] = dot2(V[3 * (a) + 1][k], mh, acc[9 + 2 * k]); \
      acc[16 + 2 * k] = dot2(V[3 * (a) + 2][k], ol, acc[16 + 2 * k]); acc[17 + 2 * k] = dot2(V[3 * (a) + 2][k], oh, acc[17 + 2 * k]); } \
    SCHED(); }
  PV(0) PV(1) PV(2) PV(3) PV(4) PV(5)
  {
#pragma unroll 1
    for (unsigned spins = 0; MISC[2] < 8u && spins < (SPIN_CAP << 4); ++spins) __builtin_amdgcn_s_sleep(1);
    const float m = u2f(MISC[16 + (c >> 1)]);
    fsc = __expf(m - row_max(m));
  }
  SCHED();
  PV(6) PV(7)
#undef PV
  STAMP(7);
  {
    float u[12], z[6];
#pragma unroll
    for (int k = 0; k < 12; ++k) { auto r = __builtin_amdgcn_permlane32_swap(f2u(acc[k]), f2u(acc[k + 12]), false, false); u[k] = u2f(r[0]) + u2f(r[1]); }
#pragma unroll
    for (int k = 0; k < 6; ++k) { auto r = __builtin_amdgcn_permlane16_swap(f2u(u[k]), f2u(u[k + 6]), false, false); z[k] = u2f(r[0]) + u2f(r[1]); }
    LAS float* op = (LAS float*)(L + L_OP + w * 1552 + g * 384 + c * 4);
#pragma unroll
    for (int k = 0; k < 6; ++k) op[k * 16] = z[k];
  }
  wg_barrier();
  {
    float ov[6];
#pragma unroll
    for (int k = 0; k < 6; ++k) {
      const int d = 24 * w + 6 * g + k, ck = 24 * (c & 1) + (d >> 3);
      ov[k] = *(LAS float*)(L + L_OP + (c >> 1) * 1552 + ((ck >> 4) * 8 + (d & 7)) * 64 + (ck & 15) * 4) * fsc;
    }
#pragma unroll
    for (int k = 0; k < 6; ++k) ov[k] = row_sum(ov[k]);
    const float lo = c == 0 ? ov[0] : c == 1 ? ov[2] : ov[4], hi = c == 0 ? ov[1] : c == 1 ? ov[3] : ov[5];
    if (c < 3) stsc(rws, WS_OG + ((h * 8 + j8) * 96 + 12 * w + 3 * g + c) * 16, u32x4{f2u(lo), tag, f2u(hi), tag});
  }
  STAMP(8);
  if (w < 6) {
    const int ob = tid / 96, ou = tid % 96;
    const int o0 = WS_OG + ((h * 8 + 2 * ob) * 96 + ou) * 16, m0 = WS_ML + (h * 8 + 2 * ob) * 16;
    u32x4 t0, t1, ma, mb;
#pragma unroll 1
    for (unsigned spins = 0; spins < SPIN_CAP; ++spins) {
      MEMBAR();
      t0 = ldsc(rws, o0); t1 = ldsc(rws, o0 + 1536); ma = ldsc(rws, m0); mb = ldsc(rws, m0 + 16);
      if (all0(tag2(t0, tag) | tag2(t1, tag) | tag2(ma, tag) | tag2(mb, tag))) break;
      __builtin_amdgcn_s_sleep(1);
    }
    const float M = fmaxf(u2f(ma.x), u2f(mb.x)), e0 = __expf(u2f(ma.x) - M), e1 = __expf(u2f(mb.x) - M);
    const float inv = 1.f / (u2f(ma.z) * e0 + u2f(mb.z) * e1), w0 = e0 * inv, w1 = e1 * inv;
    *(LAS unsigned*)(L + L_OA + ob * 448 + ou * 4) = pack2(u2f(t0.x) * w0 + u2f(t1.x) * w1, u2f(t0.z) * w0 + u2f(t1.z) * w1);
  }
  wg_barrier();
  STAMP(9);
  if (w < 5) {
    f32x4 y = {0, 0, 0, 0};
    LAS unsigned char* pa = L + L_OA + (c & 3) * 448 + g * 16;
#pragma unroll
    for (int ks = 0; ks < 6; ++ks) y = mfma32(*(LAS u32x4*)(pa + ks * 64), BO[ks], y);
    stsc8(rws, WS_YG + ((g * 40 + 5 * j8 + w) * 32 + h) * 128 + c * 8, u32x2{f2u(sel4(y, g)), tag});
  }
  STAMP(10);
  if (isRed) {
    float x1a = 0.f, x1b = 0.f; unsigned gb2 = 0;
    if (w == 0) {
      u32x4 t[4]; u32x2 ga, gb;
      const int yo = WS_YG + (rb * 40 + rt) * 4096 + lane * 16, mo = WS_MG + (rb * 1280 + 8 * rt + (lane & 7)) * 8;
#pragma unroll 1
      for (unsigned spins = 0; spins < SPIN_CAP; ++spins) {
        MEMBAR();
#pragma unroll
        for (int k = 0; k < 4; ++k) t[k] = ldsc(rws, yo + k * 1024);
        ga = ldsc8(rws, mo); gb = ldsc8(rws, mo + 960 * 8);
        if (all0(tag2(t[0], tag) | tag2(t[1], tag) | tag2(t[2], tag) | tag2(t[3], tag) | (ga.y ^ tag) | (gb.y ^ tag))) break;
        __builtin_amdgcn_s_sleep(1);
      }
      float ya = (u2f(t[0].x) + u2f(t[1].x)) + (u2f(t[2].x) + u2f(t[3].x)), yb = (u2f(t[0].z) + u2f(t[1].z)) + (u2f(t[2].z) + u2f(t[3].z));
      ya += dpp<XOR8>(ya); yb += dpp<XOR8>(yb);
      ya = xrow_sum(ya); yb = xrow_sum(yb);
      x1a = rbf(bflo(xres) + rbf(bflo(ga.x) * rbf(ya))); x1b = rbf(bfhi(xres) + rbf(bfhi(ga.x) * rbf(yb)));
      gb2 = gb.x;
      if (lane < 8) stsc8(rws, WS_XG + (rb * 320 + 8 * rt + lane) * 8, u32x2{pack2(x1a, x1b), tag});
    }
    STAMP(11);
    if (w < 6) {
      u32x4 t0, t1;
      const int po = WS_PG + (rb * 40 + rt) * 12288 + tid * 16;
#pragma unroll 1
      for (unsigned spins = 0; spins < SPIN_CAP; ++spins) {
        MEMBAR();
        t0 = ldsc(rws, po); t1 = ldsc(rws, po + 6144);
        if (all0(tag2(t0, tag) | tag2(t1, tag))) break;
        __builtin_amdgcn_s_sleep(2);
      }
      float ya = u2f(t0.x) + u2f(t1.x), yb = u2f(t0.z) + u2f(t1.z);
      ya += dpp<XOR8>(ya); yb += dpp<XOR8>(yb);
      ya = xrow_sum(ya); yb = xrow_sum(yb);
      if (lane < 8) *(LAS u32x2*)(L + L_RED + w * 64 + lane * 8) = u32x2{f2u(ya), f2u(yb)};
    }
    wg_barrier();
    if (tid < 8) {
      float ya = 0.f, yb = 0.f;
#pragma unroll
      for (int k = 0; k < 6; ++k) { const u32x2 r = *(LAS u32x2*)(L + L_RED + k * 64 + lane * 8); ya += u2f(r.x); yb += u2f(r.y); }
      ((unsigned*)out)[rb * 320 + 8 * rt + lane] = pack2(x1a + rbf(bflo(gb2) * rbf(ya)), x1b + rbf(bfhi(gb2) * rbf(yb)));
      if (bid == 0 && tid == 0) __hip_atomic_store((GAS unsigned*)(ws + WS_EPOCH), epoch + 1u, __ATOMIC_RELAXED, __HIP_MEMORY_SCOPE_AGENT);
    }
    STAMP(12);
  } else {
    u32x4 BG[5]; u32x2 BD[5];
    {
      LAS unsigned char* pb = L + L_RL + (w >> 1) * (5 * 2176) + g * 544 + trq * 64 + trp * 8 + (w & 1) * 32;
#pragma unroll
      for (int ks = 0; ks < 5; ++ks) BG[ks] = trfrag(pb + ks * 2176, 256);
      LAS unsigned char* pd = L + L_WD + (4 * g + trq) * 1312 + trp * 8 + w * 160;
#pragma unroll
      for (int t = 0; t < 5; ++t) BD[t] = trread(pd + t * 32);
    }
    if (w < 4) {
      u32x4 sb[3], cb[3], t[3];
      int oo[3];
#pragma unroll
      for (int k = 0; k < 3; ++k) oo[k] = min(lane + 64 * k, 159) * 16;
#pragma unroll 1
      for (unsigned spins = 0; spins < SPIN_CAP; ++spins) {
        MEMBAR();
        unsigned bad = 0;
#pragma unroll
        for (int k = 0; k < 3; ++k) { sb[k] = ldsc(rws, WS_MG + w * 10240 + 2560 + oo[k]); cb[k] = ldsc(rws, WS_MG + w * 10240 + 5120 + oo[k]); }
#pragma unroll
        for (int k = 0; k < 3; ++k) bad |= tag2(sb[k], tag) | tag2(cb[k], tag);
        if (all0(bad)) break;
        __builtin_amdgcn_s_sleep(2);
      }
#pragma unroll 1
      for (unsigned spins = 0; spins < SPIN_CAP; ++spins) {
        MEMBAR();
#pragma unroll
        for (int k = 0; k < 3; ++k) t[k] = ldsc(rws, WS_XG + w * 2560 + oo[k]);
        if (all0(tag2(t[0], tag) | tag2(t[1], tag) | tag2(t[2], tag))) break;
        __builtin_amdgcn_s_sleep(1);
      }
      STAMP(11);
      float xv[12];
#pragma unroll
      for (int k = 0; k < 3; ++k) { xv[4 * k] = bflo(t[k].x); xv[4 * k + 1] = bfhi(t[k].x); xv[4 * k + 2] = bflo(t[k].z); xv[4 * k + 3] = bfhi(t[k].z); }
      const bool l3 = lane < 32;
      float sm = ((xv[0] + xv[1]) + (xv[2] + xv[3])) + ((xv[4] + xv[5]) + (xv[6] + xv[7]));
      sm += l3 ? (xv[8] + xv[9]) + (xv[10] + xv[11]) : 0.f;
      const float mean = wave_sum(sm) * (1.f / D);
      float sv = 0.f, sv3 = 0.f;
#pragma unroll
      for (int k = 0; k < 8; ++k) { const float dlt = xv[k] - mean; sv = fmaf(dlt, dlt, sv); }
#pragma unroll
      for (int k = 8; k < 12; ++k) { const float dlt = xv[k] - mean; sv3 = fmaf(dlt, dlt, sv3); }
      sv += l3 ? sv3 : 0.f;
      const float var = wave_sum(sv) * (1.f / D);
      const float mb = rbf(mean), den = rbf(sqrtf(rbf(rbf(var) + 1e-5f)));
#pragma unroll
      for (int k = 0; k < 3; ++k) {
        const float s0 = bflo(sb[k].x), s1 = bfhi(sb[k].x), s2 = bflo(sb[k].z), s3 = bfhi(sb[k].z);
        const float c0 = bflo(cb[k].x), c1 = bfhi(cb[k].x), c2 = bflo(cb[k].z), c3 = bfhi(cb[k].z);
        const float h0 = rbf(rbf(rbf(xv[4 * k] - mb) / den) * rbf(1.f + c0)) + s0, h1 = rbf(rbf(rbf(xv[4 * k + 1] - mb) / den) * rbf(1.f + c1)) + s1;
        const float h2 = rbf(rbf(rbf(xv[4 * k + 2] - mb) / den) * rbf(1.f + c2)) + s2, h3 = rbf(rbf(rbf(xv[4 * k + 3] - mb) / den) * rbf(1.f + c3)) + s3;
        *(LAS u32x2*)(L + L_A + w * 1344 + (oo[k] >> 1)) = u32x2{pack2(h0, h1), pack2(h2, h3)};
      }
    }
    wg_barrier();
    {
      f32x4 a = {0, 0, 0, 0};
      LAS unsigned char* pa = L + L_A + (c & 3) * 1344 + (w >> 1) * 320 + g * 16;
#pragma unroll
      for (int ks = 0; ks < 5; ++ks) a = mfma32(*(LAS u32x4*)(pa + ks * 64), BG[ks], a);
      *(LAS float*)(L + L_RED + w * 256 + lane * 4) = sel4(a, g);
    }
    wg_barrier();
    {
      f32x4 gs = {0, 0, 0, 0}, us = {0, 0, 0, 0};
#pragma unroll
      for (int k = 0; k < 4; ++k) {
        gs += *(LAS f32x4*)(L + L_RED + (2 * k) * 256 + (c & 3) * 64 + g * 16);
        us += *(LAS f32x4*)(L + L_RED + (2 * k + 1) * 256 + (c & 3) * 64 + g * 16);
      }
      float hh[4];
#pragma unroll
      for (int k = 0; k < 4; ++k) { const float gv = rbf(gs[k]); hh[k] = gv * (gv > 0.f ? 1.f : 0.f) * rbf(us[k]); }
      const u32x2 af = {pack2(hh[0], hh[1]), pack2(hh[2], hh[3])};
#pragma unroll
      for (int t = 0; t < 5; ++t) {
        f32x4 y = {0, 0, 0, 0};
        y = __builtin_amdgcn_mfma_f32_16x16x16bf16_1k(__builtin_bit_cast(s16x4, af), __builtin_bit_cast(s16x4, BD[t]), y, 0, 0, 0);
        stsc8(rws, WS_PG + ((g * 40 + 5 * w + t) * 96 + mid) * 128 + c * 8, u32x2{f2u(sel4(y, g)), tag});
      }
    }
    STAMP(12);
  }
}

extern "C" void kernel_launch(void* const* d_in, const int* in_sizes, int n_in, void* d_out, int out_size, void* d_ws, size_t ws_size, hipStream_t stream) {
  fused_block<<<256, 512, 0, stream>>>((const u16*)d_in[0], (const u16*)d_in[1], (const u16*)d_in[2], (const u16*)d_in[3], (const u16*)d_in[4],
                                       (const u16*)d_in[6], (const u16*)d_in[7], (const u16*)d_in[8], (const u16*)d_in[9], (const u16*)d_in[10],
                                       (u16*)d_out, (unsigned char*)d_ws);
}
```

```cpp
#define AUX_K 2
#define AUX_V 2
#define AUX_W 0
#include <hip/hip_runtime.h>
#include <hip/hip_bf16.h>
#include <stdint.h>

#define W_WIN 5
#ifndef NV0
#define NV0 9
#endif
#define STAMPS 0

typedef unsigned short u16;
typedef unsigned u32x4 __attribute__((ext_vector_type(4)));
typedef unsigned u32x2 __attribute__((ext_vector_type(2)));
typedef float f32x4 __attribute__((ext_vector_type(4)));
typedef short s16x4 __attribute__((ext_vector_type(4)));
typedef short s16x8 __attribute__((ext_vector_type(8)));
typedef __bf16 bf16x2 __attribute__((ext_vector_type(2)));
typedef __bf16 bf16x8 __attribute__((ext_vector_type(8)));
#define LAS __attribute__((address_space(3)))
#define GAS __attribute__((address_space(1)))
typedef __amdgpu_buffer_rsrc_t rsrc_t;

constexpr int D = 640, NH = 32, DH = 192, SK = 4096, WIN = 1024, FF = 1536;
constexpr int AUX_NT = 2, AUX_SC1 = 16;
constexpr unsigned SPIN_CAP = 1u << 16;

constexpr int WS_EPOCH = 0;
constexpr int WS_HG = 1024;
constexpr int WS_XG = WS_HG + 4 * 320 * 8;
constexpr int WS_MG = WS_XG + 4 * 320 * 8;
constexpr int WS_ML = WS_MG + 4 * 1280 * 8;
constexpr int WS_OG = WS_ML + 256 * 16;
constexpr int WS_QG = WS_OG + 256 * 96 * 16;
constexpr int WS_YG = WS_QG + 32 * 4 * 384 * 16;
constexpr int WS_PG = WS_YG + 4 * 40 * 32 * 128;
constexpr int WS_END = WS_PG + 4 * 40 * 96 * 128;
constexpr int WS_STAMP = 8 << 20;

constexpr int L_WQ = 0;
constexpr int L_WO = L_WQ + 160 * 192 + 20 * 32;
constexpr int L_RL = L_WO + 192 * 160 + 24 * 128;
constexpr int L_WD = L_RL + 80 * 544;
constexpr int L_A = L_WD + 16 * 1312;
constexpr int L_HA = L_A + 4 * 1344;
constexpr int L_OA = L_HA + 4 * 320;
constexpr int L_Q = L_OA + 4 * 448;
constexpr int L_OP = L_Q + 384;
constexpr int L_RED = L_OP + 8 * 1552;
constexpr int L_MISC = L_RED + 8 * 256;
constexpr int L_BYTES = L_MISC + 256;
static_assert(L_BYTES <= 160 * 1024 && L_BYTES > 80 * 1024, "one workgroup per CU");

__device__ __forceinline__ float bflo(unsigned u) { return __builtin_bit_cast(float, u << 16); }
__device__ __forceinline__ float bfhi(unsigned u) { return __builtin_bit_cast(float, u & 0xffff0000u); }
__device__ __forceinline__ float rbf(float f) { return (float)(__bf16)f; }
__device__ __forceinline__ unsigned pack2(float lo, float hi) { bf16x2 v = {(__bf16)lo, (__bf16)hi}; return __builtin_bit_cast(unsigned, v); }
__device__ __forceinline__ unsigned f2u(float f) { return __builtin_bit_cast(unsigned, f); }
__device__ __forceinline__ float u2f(unsigned u) { return __builtin_bit_cast(float, u); }
__device__ __forceinline__ float dot2(unsigned a, unsigned b, float c) {
  return __builtin_amdgcn_fdot2_f32_bf16(__builtin_bit_cast(bf16x2, a), __builtin_bit_cast(bf16x2, b), c, false);
}
template <int CTRL> __device__ __forceinline__ float dpp(float x) { return u2f((unsigned)__builtin_amdgcn_mov_dpp((int)f2u(x), CTRL, 0xf, 0xf, true)); }
template <int CTRL> __device__ __forceinline__ unsigned dppu(unsigned x) { return (unsigned)__builtin_amdgcn_mov_dpp((int)x, CTRL, 0xf, 0xf, true); }
constexpr int XOR1 = 0xB1, XOR2 = 0x4E, XOR7 = 0x141, XOR8 = 0x128, XOR15 = 0x140;
__device__ __forceinline__ float row_sum(float v) { v += dpp<XOR1>(v); v += dpp<XOR2>(v); v += dpp<XOR7>(v); v += dpp<XOR8>(v); return v; }
__device__ __forceinline__ float row_max(float v) { v = fmaxf(v, dpp<XOR1>(v)); v = fmaxf(v, dpp<XOR2>(v)); v = fmaxf(v, dpp<XOR7>(v)); v = fmaxf(v, dpp<XOR8>(v)); return v; }
__device__ __forceinline__ float xrow_sum(float x) {
  auto s = __builtin_amdgcn_permlane16_swap(f2u(x), f2u(x), false, false);
  x = u2f(s[0]) + u2f(s[1]);
  auto t = __builtin_amdgcn_permlane32_swap(f2u(x), f2u(x), false, false);
  return u2f(t[0]) + u2f(t[1]);
}
__device__ __forceinline__ float xrow_max(float x) {
  auto s = __builtin_amdgcn_permlane16_swap(f2u(x), f2u(x), false, false);
  x = fmaxf(u2f(s[0]), u2f(s[1]));
  auto t = __builtin_amdgcn_permlane32_swap(f2u(x), f2u(x), false, false);
  return fmaxf(u2f(t[0]), u2f(t[1]));
}
__device__ __forceinline__ float wave_sum(float v) { return xrow_sum(row_sum(v)); }
__device__ __forceinline__ rsrc_t mkrs(const void* p, unsigned bytes) { return __builtin_amdgcn_make_buffer_rsrc((void*)p, 0, bytes, 0x00020000); }
template <int AUX> __device__ __forceinline__ u32x4 ldx(rsrc_t rs, int voff, int soff) { return __builtin_amdgcn_raw_buffer_load_b128(rs, voff, soff, AUX); }
#define ldnt ldx<AUX_W>
__device__ __forceinline__ u32x4 ldsc(rsrc_t rs, int voff) { return __builtin_amdgcn_raw_buffer_load_b128(rs, voff, 0, AUX_SC1); }
__device__ __forceinline__ u32x2 ldsc8(rsrc_t rs, int voff) { return __builtin_amdgcn_raw_buffer_load_b64(rs, voff, 0, AUX_SC1); }
__device__ __forceinline__ void stsc(rsrc_t rs, int voff, u32x4 v) { __builtin_amdgcn_raw_buffer_store_b128(v, rs, voff, 0, AUX_SC1); }
__device__ __forceinline__ void stsc8(rsrc_t rs, int voff, u32x2 v) { __builtin_amdgcn_raw_buffer_store_b64(v, rs, voff, 0, AUX_SC1); }
template <int N> __device__ __forceinline__ void waitvm() { asm volatile("s_waitcnt vmcnt(%0)" ::"i"(N) : "memory"); }
template <int N> __device__ __forceinline__ void pace(int w) {
  asm volatile("s_cmp_eq_u32 %0, 0\n\ts_cbranch_scc1 1f\n\ts_waitcnt vmcnt(%1)\n1:" ::"s"(w), "i"(N) : "scc", "memory");
}
#define SCHED() __builtin_amdgcn_sched_barrier(0)
#define MEMBAR() asm volatile("" ::: "memory")
__device__ __forceinline__ void wg_barrier() { asm volatile("s_waitcnt lgkmcnt(0)\n\ts_barrier" ::: "memory"); }
__device__ __forceinline__ f32x4 mfma32(u32x4 a, u32x4 b, f32x4 c) {
  return __builtin_amdgcn_mfma_f32_16x16x32_bf16(__builtin_bit_cast(bf16x8, a), __builtin_bit_cast(bf16x8, b), c, 0, 0, 0);
}
__device__ __forceinline__ u32x2 trread(LAS unsigned char* p) { return __builtin_bit_cast(u32x2, __builtin_amdgcn_ds_read_tr16_b64_v4i16((LAS s16x4*)p)); }
__device__ __forceinline__ u32x4 trfrag(LAS unsigned char* p, int step) {
  const u32x2 lo = trread(p), hi = trread(p + step);
  return u32x4{lo.x, lo.y, hi.x, hi.y};
}
__device__ __forceinline__ float sel4(f32x4 v, int g) { const float a = (g & 1) ? v[1] : v[0], b = (g & 1) ? v[3] : v[2]; return (g & 2) ? b : a; }
__device__ __forceinline__ unsigned tag2(u32x4 v, unsigned tag) { return (v.y ^ tag) | (v.w ^ tag); }
__device__ __forceinline__ bool all0(unsigned bad) { return __builtin_amdgcn_ballot_w64(bad != 0u) == 0ull; }

#define STAMP(n) do {} while (0)

__global__ void __launch_bounds__(512, 2)
fused_block(const u16* __restrict__ xin, const u16* __restrict__ kc, const u16* __restrict__ vc, const u16* __restrict__ wq,
            const u16* __restrict__ wo, const u16* __restrict__ wg, const u16* __restrict__ wu, const u16* __restrict__ wd,
            const u16* __restrict__ cin, const u16* __restrict__ wmod, u16* __restrict__ out, unsigned char* ws) {
  __shared__ __attribute__((aligned(16))) unsigned char smem[L_BYTES];
  LAS unsigned char* L = (LAS unsigned char*)smem;
  volatile LAS unsigned* MISC = (volatile LAS unsigned*)(L + L_MISC);
  const int tid = threadIdx.x, lane = tid & 63, w = __builtin_amdgcn_readfirstlane(tid >> 6), g = lane >> 4, c = lane & 15;
  const int bid = blockIdx.x, x = bid & 7, slot = bid >> 3, h = 4 * x + (slot >> 3), j8 = slot & 7, b = j8 >> 1, s = j8 & 1;
  const bool isMod = slot < 15, isHU = isMod && x < 4 && slot < 10, isRed = slot < 20, isMlp = slot >= 20;
  const int hu = 10 * x + slot;
  const int ru = x < 4 ? 5 * x + (slot - 10) : 20 + 15 * (x - 4) + slot;
  const int rid = x * 20 + slot, rb = rid / 40, rt = rid % 40;
  const int mid = x * 12 + (slot - 20);
  STAMP(0);

  const rsrc_t rws = mkrs(ws, WS_END);
  const size_t kvbase = ((size_t)(b * NH + h) * SK + (SK - WIN) + s * 512 + w * 64) * DH;
  const rsrc_t rk = mkrs(kc + kvbase, 64 * 384), rv = mkrs(vc + kvbase, 64 * 384);
  const int kvo = g * 768 + c * 16;
  u32x4 K[24], V[24];
#define LDK(n) K[n] = ldx<AUX_K>(rk, kvo + ((n) % 3) * 256, ((n) / 3) * 3072)
#define LDV(n) V[n] = ldx<AUX_V>(rv, kvo + ((n) % 3) * 256, ((n) / 3) * 3072)

  u32x4 SR[8], SQ[4], SO[4];
  if (isMod) {
    const rsrc_t rm = mkrs(wmod, D * 6 * D * 2);
    const int c0 = isHU ? 16 * hu : 1280 + 32 * ru, c1 = isHU ? 640 + 16 * hu : 1296 + 32 * ru;
    const int ch = tid & 3, vo = (tid >> 2) * 7680 + ((ch & 2) ? c1 : c0) * 2 + (ch & 1) * 16;
#pragma unroll
    for (int k = 0; k < 5; ++k) SR[k] = ldnt(rm, vo, k * 128 * 7680);
  } else if (isMlp) {
    const rsrc_t rg = mkrs(w < 4 ? wg : wu, D * FF * 2), rd = mkrs(wd + (size_t)mid * 16 * D, 16 * D * 2);
    const int t2 = tid & 255, vo = (t2 >> 1) * 3072 + mid * 32 + (t2 & 1) * 16;
#pragma unroll
    for (int k = 0; k < 5; ++k) SR[k] = ldnt(rg, vo, k * 128 * 3072);
#pragma unroll
    for (int k = 0; k < 3; ++k) SR[5 + k] = ldnt(rd, tid * 16 + k * 8192, 0);
  }
  {
    const rsrc_t rq = mkrs(wq + (size_t)(160 * b) * (NH * DH) + DH * h + 96 * s, 160 * NH * DH * 2 - (DH * h + 96 * s) * 2);
    const rsrc_t ro = mkrs(wo + (size_t)(DH * h) * D + 80 * j8, DH * D * 2 - 80 * j8 * 2);
#pragma unroll
    for (int k = 0; k < 4; ++k) { const int u = tid + 512 * k, row = u / 12, ch = u % 12; SQ[k] = ldnt(rq, row * 12288 + ch * 16, 0); }
#pragma unroll
    for (int k = 0; k < 4; ++k) { const int u = tid + 512 * k, row = u / 10, ch = u % 10; SO[k] = ldnt(ro, row * 1280 + ch * 16, 0); }
  }
  SCHED();
  unsigned epoch = 0;
  if (w == 0) epoch = __hip_atomic_load((GAS unsigned*)(ws + WS_EPOCH), __ATOMIC_RELAXED, __HIP_MEMORY_SCOPE_AGENT);
  u32x4 cv = {0, 0, 0, 0};
  unsigned xr[5] = {0, 0, 0, 0, 0};
  unsigned xh = 0;
  if (isMod && tid < 320) cv = *(const u32x4*)(cin + tid * 8);
  if (isHU) {
    if (w >= 4) {
#pragma unroll
      for (int k = 0; k < 5; ++k) xr[k] = ((const unsigned*)xin)[(w - 4) * 320 + lane + 64 * k];
    }
    if (w == 0) xh = ((const unsigned*)xin)[g * 320 + 8 * hu + (c >> 1)];
  }
  unsigned xres = 0;
  if (isRed && tid < 8) xres = ((const unsigned*)xin)[rb * 320 + rt * 8 + tid];
  SCHED();
  auto prologue_finish = [&]() {
    if (tid == 0) { MISC[0] = (epoch + 1u) * 0x9E3779B1u; MISC[1] = 0u; MISC[2] = 0u; }
    if (isMod && tid < 320) {
      u32x4 o;
#pragma unroll
      for (int k = 0; k < 4; ++k) { const float a = bflo(cv[k]), bb = bfhi(cv[k]); o[k] = pack2(a / (1.f + __expf(-a)), bb / (1.f + __expf(-bb))); }
      *(LAS u32x4*)(L + L_A + (tid / 80) * 1344 + (tid % 80) * 16) = o;
    }
    if (isHU && w >= 4) {
      float sm = 0.f;
#pragma unroll
      for (int k = 0; k < 5; ++k) sm += bflo(xr[k]) + bfhi(xr[k]);
      const float mean = wave_sum(sm) * (1.f / D);
      float sv = 0.f;
#pragma unroll
      for (int k = 0; k < 5; ++k) { const float a = bflo(xr[k]) - mean, bb = bfhi(xr[k]) - mean; sv += a * a + bb * bb; }
      const float var = wave_sum(sv) * (1.f / D);
      if (lane == 0) { MISC[4 + 2 * (w - 4)] = f2u(rbf(mean)); MISC[5 + 2 * (w - 4)] = f2u(rbf(sqrtf(rbf(rbf(var) + 1e-5f)))); }
    }
    if (isMod) {
#pragma unroll
      for (int k = 0; k < 5; ++k) { const int row = (tid >> 2) + 128 * k; *(LAS u32x4*)(L + L_RL + row * 64 + (row >> 3) * 32 + (tid & 3) * 16) = SR[k]; }
    } else if (isMlp) {
      const int t2 = tid & 255;
#pragma unroll
      for (int k = 0; k < 5; ++k) { const int row = (t2 >> 1) + 128 * k; *(LAS u32x4*)(L + L_RL + row * 64 + (row >> 3) * 32 + (w >> 2) * 32 + (t2 & 1) * 16) = SR[k]; }
#pragma unroll
      for (int k = 0; k < 3; ++k) { const int u = tid + 512 * k; if (u < 1280) *(LAS u32x4*)(L + L_WD + (u / 80) * 1312 + (u % 80) * 16) = SR[5 + k]; }
    }
#pragma unroll
    for (int k = 0; k < 4; ++k) { const int u = tid + 512 * k, row = u / 12, ch = u % 12; if (u < 1920) *(LAS u32x4*)(L + L_WQ + row * 192 + (row >> 3) * 32 + ch * 16) = SQ[k]; }
#pragma unroll
    for (int k = 0; k < 4; ++k) { const int u = tid + 512 * k, row = u / 10, ch = u % 10; if (u < 1920) *(LAS u32x4*)(L + L_WO + row * 160 + (row >> 3) * 128 + ch * 16) = SO[k]; }
  };
  const int trq = (c >> 2), trp = (c & 3);

#pragma unroll
  for (int n = 0; n < W_WIN; ++n) LDK(n);
  SCHED();
  prologue_finish();
  wg_barrier();
  STAMP(1);
  const unsigned tag = MISC[0];
#pragma unroll
  for (int n = W_WIN; n < 24; ++n) { pace<W_WIN - 1>(w); LDK(n); SCHED(); }
  if (w == 0) {
    if (isMod) {
      f32x4 a0 = {0, 0, 0, 0}, a1 = {0, 0, 0, 0};
      LAS unsigned char* pa = L + L_A + (c & 3) * 1344 + g * 16;
      LAS unsigned char* pb = L + L_RL + g * 544 + trq * 64 + trp * 8;
#pragma unroll
      for (int ks = 0; ks < 20; ++ks) {
        const u32x4 af = *(LAS u32x4*)(pa + ks * 64);
        a0 = mfma32(af, trfrag(pb + ks * 2176, 256), a0);
        a1 = mfma32(af, trfrag(pb + ks * 2176 + 32, 256), a1);
      }
      const float v0 = rbf(sel4(a0, g)), v1 = rbf(sel4(a1, g));
      if (isHU) {
        const float mb = u2f(MISC[4 + 2 * g]), den = u2f(MISC[5 + 2 * g]);
        const float hv = rbf(rbf(rbf(rbf(((c & 1) ? bfhi(xh) : bflo(xh)) - mb) / den) * rbf(1.f + v1)) + v0);
        const float hn = dpp<XOR1>(hv);
        if ((c & 1) == 0) stsc8(rws, WS_HG + (g * 320 + 8 * hu + (c >> 1)) * 8, u32x2{pack2(hv, hn), tag});
      } else {
        const float n0 = dpp<XOR1>(v0), n1 = dpp<XOR1>(v1);
        const unsigned pv = (c & 1) ? pack2(n1, v1) : pack2(v0, n0);
        stsc8(rws, WS_MG + (g * 1280 + 16 * ru + (c & 1) * 8 + (c >> 1)) * 8, u32x2{pv, tag});
      }
    }
    STAMP(2);
    {
      u32x4 t[3];
      int off[3];
#pragma unroll
      for (int k = 0; k < 3; ++k) { const int n = min(lane + 64 * k, 159); off[k] = WS_HG + (n / 40) * 2560 + b * 640 + (n % 40) * 16; }
#pragma unroll 1
      for (unsigned spins = 0; spins < SPIN_CAP; ++spins) {
        MEMBAR();
#pragma unroll
        for (int k = 0; k < 3; ++k) t[k] = ldsc(rws, off[k]);
        if (all0(tag2(t[0], tag) | tag2(t[1], tag) | tag2(t[2], tag))) break;
        __builtin_amdgcn_s_sleep(4);
      }
#pragma unroll
      for (int k = 0; k < 3; ++k) { const int n = min(lane + 64 * k, 159); *(LAS u32x2*)(L + L_HA + (n / 40) * 320 + (n % 40) * 8) = u32x2{t[k].x, t[k].z}; }
    }
    STAMP(3);
    {
      f32x4 acc[6];
#pragma unroll
      for (int t = 0; t < 6; ++t) acc[t] = f32x4{0, 0, 0, 0};
      LAS unsigned char* pa = L + L_HA + (c & 3) * 320 + g * 16;
      LAS unsigned char* pb = L + L_WQ + g * 1568 + trq * 192 + trp * 8;
#pragma unroll
      for (int ks = 0; ks < 5; ++ks) {
        const u32x4 af = *(LAS u32x4*)(pa + ks * 64);
#pragma unroll
        for (int t = 0; t < 6; ++t) acc[t] = mfma32(af, trfrag(pb + ks * 6272 + t * 32, 768), acc[t]);
      }
#pragma unroll
      for (int tp = 0; tp < 3; ++tp)
        stsc(rws, WS_QG + ((((h * 4 + g) * 4 + b) * 2 + s) * 48 + tp * 16 + c) * 16, u32x4{f2u(sel4(acc[2 * tp], g)), tag, f2u(sel4(acc[2 * tp + 1], g)), tag});
    }
    STAMP(4);
    {
      u32x4 t[6];
      const int qb = WS_QG + (h * 4 + b) * 384 * 16;
      const int o1 = qb + lane * 16, o2 = qb + ((lane >> 5) * 192 + 64 + (lane & 31)) * 16;
#pragma unroll 1
      for (unsigned spins = 0; spins < SPIN_CAP; ++spins) {
        MEMBAR();
#pragma unroll
        for (int k = 0; k < 4; ++k) t[k] = ldsc(rws, o1 + k * 1536);
#pragma unroll
        for (int k = 0; k < 2; ++k) t[4 + k] = ldsc(rws, o2 + k * 1536);
        unsigned bad = 0;
#pragma unroll
        for (int k = 0; k < 6; ++k) bad |= tag2(t[k], tag);
        if (all0(bad)) break;
        __builtin_amdgcn_s_sleep(4);
      }
      const float qa = (u2f(t[0].x) + u2f(t[1].x)) + (u2f(t[2].x) + u2f(t[3].x)), qb2 = (u2f(t[0].z) + u2f(t[1].z)) + (u2f(t[2].z) + u2f(t[3].z));
      float qc = u2f(t[4].x) + u2f(t[5].x), qd = u2f(t[4].z) + u2f(t[5].z);
      { auto r = __builtin_amdgcn_permlane32_swap(f2u(qc), f2u(qc), false, false); qc = u2f(r[0]) + u2f(r[1]); }
      { auto r = __builtin_amdgcn_permlane32_swap(f2u(qd), f2u(qd), false, false); qd = u2f(r[0]) + u2f(r[1]); }
      LAS u16* Q = (LAS u16*)(L + L_Q);
      const int d1 = lane < 48 ? 32 * g + c : 96 + c;
      Q[d1] = (u16)pack2(qa, 0.f); Q[d1 + 16] = (u16)pack2(qb2, 0.f);
      const int d2 = 128 + 32 * (g & 1) + c;
      if (lane < 32) { Q[d2] = (u16)pack2(qc, 0.f); Q[d2 + 16] = (u16)pack2(qd, 0.f); }
      asm volatile("s_waitcnt lgkmcnt(0)" ::: "memory");
      MISC[1] = 1u;
    }
  }
  SCHED();
#pragma unroll
  for (int n = 0; n < NV0; ++n) { pace<W_WIN - 1>(w); LDV(n); SCHED(); }
  STAMP(5);
#pragma unroll 1
  for (unsigned spins = 0; MISC[1] == 0u && spins < (SPIN_CAP << 4); ++spins) __builtin_amdgcn_s_sleep(2);
  STAMP(13);
  SCHED();
#pragma unroll
  for (int n = NV0; n < 24; ++n) LDV(n);
  SCHED();

  unsigned pl;
  {
    LAS unsigned char* pq = L + L_Q;
    const u32x4 q0 = *(LAS u32x4*)(pq + c * 16), q1 = *(LAS u32x4*)(pq + ((16 + c) % 24) * 16), q2 = *(LAS u32x4*)(pq + (8 + c) * 16);
    float v[16];
#pragma unroll
    for (int a = 0; a < 8; ++a) {
      float d1 = 0.f;
#pragma unroll
      for (int k = 0; k < 4; ++k) d1 = dot2(K[3 * a + 1][k], q1[k], d1);
      float e = c < 8 ? d1 : 0.f, o = c < 8 ? 0.f : d1;
#pragma unroll
      for (int k = 0; k < 4; ++k) { e = dot2(K[3 * a][k], q0[k], e); o = dot2(K[3 * a + 2][k], q2[k], o); }
      v[2 * a] = e; v[2 * a + 1] = o;
    }
#define TR_STEP(HALF, CTRL) _Pragma("unroll") for (int k = 0; k < (HALF); ++k) { \
      const float t1 = v[k] + dpp<CTRL>(v[k]), t2 = v[k + (HALF)] + dpp<CTRL>(v[k + (HALF)]); v[k] = (c & (HALF)) ? t2 : t1; }
    TR_STEP(8, XOR8) TR_STEP(4, XOR7) TR_STEP(2, XOR2) TR_STEP(1, XOR1)
#undef TR_STEP
    const float sc = rbf(rbf(v[0]) * 0.07216878364870322f);
    const float mw = xrow_max(row_max(sc));
    const float p = __expf(sc - mw);
    const float lw = wave_sum(p);
    pl = pack2(p, 0.f);
    if (lane == 0) { MISC[16 + w] = f2u(mw); MISC[24 + w] = f2u(lw); }
    asm volatile("s_waitcnt lgkmcnt(0)" ::: "memory");
    unsigned old = 0;
    if (lane == 0) old = __hip_atomic_fetch_add((LAS unsigned*)(L + L_MISC + 8), 1u, __ATOMIC_RELAXED, __HIP_MEMORY_SCOPE_WORKGROUP);
    if (__builtin_amdgcn_readfirstlane(old) == 7u) {
      const float m = u2f(MISC[16 + (c & 7)]), l = u2f(MISC[24 + (c & 7)]);
      const float M = row_max(m);
      const float Ls = row_sum(l * __expf(m - M)) * 0.5f;
      if (lane == 0) stsc(rws, WS_ML + (h * 8 + j8) * 16, u32x4{f2u(M), tag, f2u(Ls), tag});
    }
  }
  STAMP(6);
  SCHED();
  u32x4 BO[6];
  if (w < 5) {
    LAS unsigned char* pb = L + L_WO + g * 1408 + trq * 160 + trp * 8 + w * 32;
#pragma unroll
    for (int ks = 0; ks < 6; ++ks) BO[ks] = trfrag(pb + ks * 5632, 640);
  }
  float acc[24];
#pragma unroll
  for (int k = 0; k < 24; ++k) acc[k] = 0.f;
  const unsigned ph = pl << 16;
  float fsc = 0.f;
#define PV(a) { \
    const unsigned el = dppu<0x150 + 2 * (a)>(pl), ol = dppu<0x151 + 2 * (a)>(pl), eh = dppu<0x150 + 2 * (a)>(ph), oh = dppu<0x151 + 2 * (a)>(ph); \
    const unsigned ml = c < 8 ? el : ol, mh = c < 8 ? eh : oh; \
    _Pragma("unroll") for (int k = 0; k < 4; ++k) { \
      acc[2 * k] = dot2(V[3 * (a)][k], el, acc[2 * k]); acc[2 * k + 1] = dot2(V[3 * (a)][k], eh, acc[2 * k + 1]); \
      acc[8 + 2 * k] = dot2(V[3 * (a) + 1][k], ml, acc[8 + 2 * k]); acc[9 + 2 * k] = dot2(V[3 * (a) + 1][k], mh, acc[9 + 2 * k]); \
      acc[16 + 2 * k] = dot2(V[3 * (a) + 2][k], ol, acc[16 + 2 * k]); acc[17 + 2 * k] = dot2(V[3 * (a) + 2][k], oh, acc[17 + 2 * k]); } \
    SCHED(); }
  PV(0) PV(1) PV(2) PV(3) PV(4) PV(5)
  {
#pragma unroll 1
    for (unsigned spins = 0; MISC[2] < 8u && spins < (SPIN_CAP << 4); ++spins) __builtin_amdgcn_s_sleep(1);
    const float m = u2f(MISC[16 + (c >> 1)]);
    fsc = __expf(m - row_max(m));
  }
  SCHED();
  PV(6) PV(7)
#undef PV
  STAMP(7);
  {
    float u[12], z[6];
#pragma unroll
    for (int k = 0; k < 12; ++k) { auto r = __builtin_amdgcn_permlane32_swap(f2u(acc[k]), f2u(acc[k + 12]), false, false); u[k] = u2f(r[0]) + u2f(r[1]); }
#pragma unroll
    for (int k = 0; k < 6; ++k) { auto r = __builtin_amdgcn_permlane16_swap(f2u(u[k]), f2u(u[k + 6]), false, false); z[k] = u2f(r[0]) + u2f(r[1]); }
    LAS float* op = (LAS float*)(L + L_OP + w * 1552 + g * 384 + c * 4);
#pragma unroll
    for (int k = 0; k < 6; ++k) op[k * 16] = z[k];
  }
  wg_barrier();
  {
    float ov[6];
#pragma unroll
    for (int k = 0; k < 6; ++k) {
      const int d = 24 * w + 6 * g + k, ck = 24 * (c & 1) + (d >> 3);
      ov[k] = *(LAS float*)(L + L_OP + (c >> 1) * 1552 + ((ck >> 4) * 8 + (d & 7)) * 64 + (ck & 15) * 4) * fsc;
    }
#pragma unroll
    for (int k = 0; k < 6; ++k) ov[k] = row_sum(ov[k]);
    const float lo = c == 0 ? ov[0] : c == 1 ? ov[2] : ov[4], hi = c == 0 ? ov[1] : c == 1 ? ov[3] : ov[5];
    if (c < 3) stsc(rws, WS_OG + ((h * 8 + j8) * 96 + 12 * w + 3 * g + c) * 16, u32x4{f2u(lo), tag, f2u(hi), tag});
  }
  STAMP(8);
  if (w < 6) {
    const int ob = tid / 96, ou = tid % 96;
    const int o0 = WS_OG + ((h * 8 + 2 * ob) * 96 + ou) * 16, m0 = WS_ML + (h * 8 + 2 * ob) * 16;
    u32x4 t0, t1, ma, mb;
#pragma unroll 1
    for (unsigned spins = 0; spins < SPIN_CAP; ++spins) {
      MEMBAR();
      t0 = ldsc(rws, o0); t1 = ldsc(rws, o0 + 1536); ma = ldsc(rws, m0); mb = ldsc(rws, m0 + 16);
      if (all0(tag2(t0, tag) | tag2(t1, tag) | tag2(ma, tag) | tag2(mb, tag))) break;
      __builtin_amdgcn_s_sleep(1);
    }
    const float M = fmaxf(u2f(ma.x), u2f(mb.x)), e0 = __expf(u2f(ma.x) - M), e1 = __expf(u2f(mb.x) - M);
    const float inv = 1.f / (u2f(ma.z) * e0 + u2f(mb.z) * e1), w0 = e0 * inv, w1 = e1 * inv;
    *(LAS unsigned*)(L + L_OA + ob * 448 + ou * 4) = pack2(u2f(t0.x) * w0 + u2f(t1.x) * w1, u2f(t0.z) * w0 + u2f(t1.z) * w1);
  }
  wg_barrier();
  STAMP(9);
  if (w < 5) {
    f32x4 y = {0, 0, 0, 0};
    LAS unsigned char* pa = L + L_OA + (c & 3) * 448 + g * 16;
#pragma unroll
    for (int ks = 0; ks < 6; ++ks) y = mfma32(*(LAS u32x4*)(pa + ks * 64), BO[ks], y);
    stsc8(rws, WS_YG + ((g * 40 + 5 * j8 + w) * 32 + h) * 128 + c * 8, u32x2{f2u(sel4(y, g)), tag});
  }
  STAMP(10);
  if (isRed) {
    float x1a = 0.f, x1b = 0.f; unsigned gb2 = 0;
    if (w == 0) {
      u32x4 t[4]; u32x2 ga, gb;
      const int yo = WS_YG + (rb * 40 + rt) * 4096 + lane * 16, mo = WS_MG + (rb * 1280 + 8 * rt + (lane & 7)) * 8;
#pragma unroll 1
      for (unsigned spins = 0; spins < SPIN_CAP; ++spins) {
        MEMBAR();
#pragma unroll
        for (int k = 0; k < 4; ++k) t[k] = ldsc(rws, yo + k * 1024);
        ga = ldsc8(rws, mo); gb = ldsc8(rws, mo + 960 * 8);
        if (all0(tag2(t[0], tag) | tag2(t[1], tag) | tag2(t[2], tag) | tag2(t[3], tag) | (ga.y ^ tag) | (gb.y ^ tag))) break;
        __builtin_amdgcn_s_sleep(1);
      }
      float ya = (u2f(t[0].x) + u2f(t[1].x)) + (u2f(t[2].x) + u2f(t[3].x)), yb = (u2f(t[0].z) + u2f(t[1].z)) + (u2f(t[2].z) + u2f(t[3].z));
      ya += dpp<XOR8>(ya); yb += dpp<XOR8>(yb);
      ya = xrow_sum(ya); yb = xrow_sum(yb);
      x1a = rbf(bflo(xres) + rbf(bflo(ga.x) * rbf(ya))); x1b = rbf(bfhi(xres) + rbf(bfhi(ga.x) * rbf(yb)));
      gb2 = gb.x;
      if (lane < 8) stsc8(rws, WS_XG + (rb * 320 + 8 * rt + lane) * 8, u32x2{pack2(x1a, x1b), tag});
    }
    STAMP(11);
    if (w < 6) {
      u32x4 t0, t1;
      const int po = WS_PG + (rb * 40 + rt) * 12288 + tid * 16;
#pragma unroll 1
      for (unsigned spins = 0; spins < SPIN_CAP; ++spins) {
        MEMBAR();
        t0 = ldsc(rws, po); t1 = ldsc(rws, po + 6144);
        if (all0(tag2(t0, tag) | tag2(t1, tag))) break;
        __builtin_amdgcn_s_sleep(2);
      }
      float ya = u2f(t0.x) + u2f(t1.x), yb = u2f(t0.z) + u2f(t1.z);
      ya += dpp<XOR8>(ya); yb += dpp<XOR8>(yb);
      ya = xrow_sum(ya); yb = xrow_sum(yb);
      if (lane < 8) *(LAS u32x2*)(L + L_RED + w * 64 + lane * 8) = u32x2{f2u(ya), f2u(yb)};
    }
    wg_barrier();
    if (tid < 8) {
      float ya = 0.f, yb = 0.f;
#pragma unroll
      for (int k = 0; k < 6; ++k) { const u32x2 r = *(LAS u32x2*)(L + L_RED + k * 64 + lane * 8); ya += u2f(r.x); yb += u2f(r.y); }
      ((unsigned*)out)[rb * 320 + 8 * rt + lane] = pack2(x1a + rbf(bflo(gb2) * rbf(ya)), x1b + rbf(bfhi(gb2) * rbf(yb)));
      if (bid == 0 && tid == 0) __hip_atomic_store((GAS unsigned*)(ws + WS_EPOCH), epoch + 1u, __ATOMIC_RELAXED, __HIP_MEMORY_SCOPE_AGENT);
    }
    STAMP(12);
  } else {
    u32x4 BG[5]; u32x2 BD[5];
    {
      LAS unsigned char* pb = L + L_RL + (w >> 1) * (5 * 2176) + g * 544 + trq * 64 + trp * 8 + (w & 1) * 32;
#pragma unroll
      for (int ks = 0; ks < 5; ++ks) BG[ks] = trfrag(pb + ks * 2176, 256);
      LAS unsigned char* pd = L + L_WD + (4 * g + trq) * 1312 + trp * 8 + w * 160;
#pragma unroll
      for (int t = 0; t < 5; ++t) BD[t] = trread(pd + t * 32);
    }
    if (w < 4) {
      u32x4 sb[3], cb[3], t[3];
      int oo[3];
#pragma unroll
      for (int k = 0; k < 3; ++k) oo[k] = min(lane + 64 * k, 159) * 16;
#pragma unroll 1
      for (unsigned spins = 0; spins < SPIN_CAP; ++spins) {
        MEMBAR();
        unsigned bad = 0;
#pragma unroll
        for (int k = 0; k < 3; ++k) { sb[k] = ldsc(rws, WS_MG + w * 10240 + 2560 + oo[k]); cb[k] = ldsc(rws, WS_MG + w * 10240 + 5120 + oo[k]); }
#pragma unroll
        for (int k = 0; k < 3; ++k) bad |= tag2(sb[k], tag) | tag2(cb[k], tag);
        if (all0(bad)) break;
        __builtin_amdgcn_s_sleep(2);
      }
#pragma unroll 1
      for (unsigned spins = 0; spins < SPIN_CAP; ++spins) {
        MEMBAR();
#pragma unroll
        for (int k = 0; k < 3; ++k) t[k] = ldsc(rws, WS_XG + w * 2560 + oo[k]);
        if (all0(tag2(t[0], tag) | tag2(t[1], tag) | tag2(t[2], tag))) break;
        __builtin_amdgcn_s_sleep(1);
      }
      STAMP(11);
      float xv[12];
#pragma unroll
      for (int k = 0; k < 3; ++k) { xv[4 * k] = bflo(t[k].x); xv[4 * k + 1] = bfhi(t[k].x); xv[4 * k + 2] = bflo(t[k].z); xv[4 * k + 3] = bfhi(t[k].z); }
      const bool l3 = lane < 32;
      float sm = ((xv[0] + xv[1]) + (xv[2] + xv[3])) + ((xv[4] + xv[5]) + (xv[6] + xv[7]));
      sm += l3 ? (xv[8] + xv[9]) + (xv[10] + xv[11]) : 0.f;
      const float mean = wave_sum(sm) * (1.f / D);
      float sv = 0.f, sv3 = 0.f;
#pragma unroll
      for (int k = 0; k < 8; ++k) { const float dlt = xv[k] - mean; sv = fmaf(dlt, dlt, sv); }
#pragma unroll
      for (int k = 8; k < 12; ++k) { const float dlt = xv[k] - mean; sv3 = fmaf(dlt, dlt, sv3); }
      sv += l3 ? sv3 : 0.f;
      const float var = wave_sum(sv) * (1.f / D);
      const float mb = rbf(mean), den = rbf(sqrtf(rbf(rbf(var) + 1e-5f)));
#pragma unroll
      for (int k = 0; k < 3; ++k) {
        const float s0 = bflo(sb[k].x), s1 = bfhi(sb[k].x), s2 = bflo(sb[k].z), s3 = bfhi(sb[k].z);
        const float c0 = bflo(cb[k].x), c1 = bfhi(cb[k].x), c2 = bflo(cb[k].z), c3 = bfhi(cb[k].z);
        const float h0 = rbf(rbf(rbf(xv[4 * k] - mb) / den) * rbf(1.f + c0)) + s0, h1 = rbf(rbf(rbf(xv[4 * k + 1] - mb) / den) * rbf(1.f + c1)) + s1;
        const float h2 = rbf(rbf(rbf(xv[4 * k + 2] - mb) / den) * rbf(1.f + c2)) + s2, h3 = rbf(rbf(rbf(xv[4 * k + 3] - mb) / den) * rbf(1.f + c3)) + s3;
        *(LAS u32x2*)(L + L_A + w * 1344 + (oo[k] >> 1)) = u32x2{pack2(h0, h1), pack2(h2, h3)};
      }
    }
    wg_barrier();
    {
      f32x4 a = {0, 0, 0, 0};
      LAS unsigned char* pa = L + L_A + (c & 3) * 1344 + (w >> 1) * 320 + g * 16;
#pragma unroll
      for (int ks = 0; ks < 5; ++ks) a = mfma32(*(LAS u32x4*)(pa + ks * 64), BG[ks], a);
      *(LAS float*)(L + L_RED + w * 256 + lane * 4) = sel4(a, g);
    }
    wg_barrier();
    {
      f32x4 gs = {0, 0, 0, 0}, us = {0, 0, 0, 0};
#pragma unroll
      for (int k = 0; k < 4; ++k) {
        gs += *(LAS f32x4*)(L + L_RED + (2 * k) * 256 + (c & 3) * 64 + g * 16);
        us += *(LAS f32x4*)(L + L_RED + (2 * k + 1) * 256 + (c & 3) * 64 + g * 16);
      }
      float hh[4];
#pragma unroll
      for (int k = 0; k < 4; ++k) { const float gv = rbf(gs[k]); hh[k] = gv * (gv > 0.f ? 1.f : 0.f) * rbf(us[k]); }
      const u32x2 af = {pack2(hh[0], hh[1]), pack2(hh[2], hh[3])};
#pragma unroll
      for (int t = 0; t < 5; ++t) {
        f32x4 y = {0, 0, 0, 0};
        y = __builtin_amdgcn_mfma_f32_16x16x16bf16_1k(__builtin_bit_cast(s16x4, af), __builtin_bit_cast(s16x4, BD[t]), y, 0, 0, 0);
        stsc8(rws, WS_PG + ((g * 40 + 5 * w + t) * 96 + mid) * 128 + c * 8, u32x2{f2u(sel4(y, g)), tag});
      }
    }
    STAMP(12);
  }
}

extern "C" void kernel_launch(void* const* d_in, const int* in_sizes, int n_in, void* d_out, int out_size, void* d_ws, size_t ws_size, hipStream_t stream) {
  fused_block<<<256, 512, 0, stream>>>((const u16*)d_in[0], (const u16*)d_in[1], (const u16*)d_in[2], (const u16*)d_in[3], (const u16*)d_in[4],
                                       (const u16*)d_in[6], (const u16*)d_in[7], (const u16*)d_in[8], (const u16*)d_in[9], (const u16*)d_in[10],
                                       (u16*)d_out, (unsigned char*)d_ws);
}
```

```cpp
#define PROBE 0
#include <hip/hip_runtime.h>
#include <hip/hip_bf16.h>
#include <stdint.h>

#define W_WIN 5
#ifndef NV0
#define NV0 9
#endif
#define AUX_K 2
#define AUX_V 2
#define AUX_W 2
#define STAMPS 0

typedef unsigned short u16;
typedef unsigned u32x4 __attribute__((ext_vector_type(4)));
typedef unsigned u32x2 __attribute__((ext_vector_type(2)));
typedef float f32x4 __attribute__((ext_vector_type(4)));
typedef short s16x4 __attribute__((ext_vector_type(4)));
typedef short s16x8 __attribute__((ext_vector_type(8)));
typedef __bf16 bf16x2 __attribute__((ext_vector_type(2)));
typedef __bf16 bf16x8 __attribute__((ext_vector_type(8)));
#define LAS __attribute__((address_space(3)))
#define GAS __attribute__((address_space(1)))
typedef __amdgpu_buffer_rsrc_t rsrc_t;

constexpr int D = 640, NH = 32, DH = 192, SK = 4096, WIN = 1024, FF = 1536;
constexpr int AUX_NT = 2, AUX_SC1 = 16;
constexpr unsigned SPIN_CAP = 1u << 16;

constexpr int WS_EPOCH = 0;
constexpr int WS_HG = 1024;
constexpr int WS_XG = WS_HG + 4 * 320 * 8;
constexpr int WS_MG = WS_XG + 4 * 320 * 8;
constexpr int WS_ML = WS_MG + 4 * 1280 * 8;
constexpr int WS_OG = WS_ML + 256 * 16;
constexpr int WS_QG = WS_OG + 256 * 96 * 16;
constexpr int WS_YG = WS_QG + 32 * 4 * 384 * 16;
constexpr int WS_PG = WS_YG + 4 * 40 * 32 * 128;
constexpr int WS_END = WS_PG + 4 * 40 * 96 * 128;
constexpr int WS_STAMP = 8 << 20;

constexpr int L_WQ = 0;
constexpr int L_WO = L_WQ + 160 * 192 + 20 * 32;
constexpr int L_RL = L_WO + 192 * 160 + 24 * 128;
constexpr int L_WD = L_RL + 80 * 544;
constexpr int L_A = L_WD + 16 * 1312;
constexpr int L_HA = L_A + 4 * 1344;
constexpr int L_OA = L_HA + 4 * 320;
constexpr int L_Q = L_OA + 4 * 448;
constexpr int L_OP = L_Q + 384;
constexpr int L_RED = L_OP + 8 * 1552;
constexpr int L_MISC = L_RED + 8 * 256;
constexpr int L_BYTES = L_MISC + 256;
static_assert(L_BYTES <= 160 * 1024 && L_BYTES > 80 * 1024, "one workgroup per CU");

__device__ __forceinline__ float bflo(unsigned u) { return __builtin_bit_cast(float, u << 16); }
__device__ __forceinline__ float bfhi(unsigned u) { return __builtin_bit_cast(float, u & 0xffff0000u); }
__device__ __forceinline__ float rbf(float f) { return (float)(__bf16)f; }
__device__ __forceinline__ unsigned pack2(float lo, float hi) { bf16x2 v = {(__bf16)lo, (__bf16)hi}; return __builtin_bit_cast(unsigned, v); }
__device__ __forceinline__ unsigned f2u(float f) { return __builtin_bit_cast(unsigned, f); }
__device__ __forceinline__ float u2f(unsigned u) { return __builtin_bit_cast(float, u); }
__device__ __forceinline__ float dot2(unsigned a, unsigned b, float c) {
  return __builtin_amdgcn_fdot2_f32_bf16(__builtin_bit_cast(bf16x2, a), __builtin_bit_cast(bf16x2, b), c, false);
}
template <int CTRL> __device__ __forceinline__ float dpp(float x) { return u2f((unsigned)__builtin_amdgcn_mov_dpp((int)f2u(x), CTRL, 0xf, 0xf, true)); }
template <int CTRL> __device__ __forceinline__ unsigned dppu(unsigned x) { return (unsigned)__builtin_amdgcn_mov_dpp((int)x, CTRL, 0xf, 0xf, true); }
constexpr int XOR1 = 0xB1, XOR2 = 0x4E, XOR7 = 0x141, XOR8 = 0x128, XOR15 = 0x140;
__device__ __forceinline__ float row_sum(float v) { v += dpp<XOR1>(v); v += dpp<XOR2>(v); v += dpp<XOR7>(v); v += dpp<XOR8>(v); return v; }
__device__ __forceinline__ float row_max(float v) { v = fmaxf(v, dpp<XOR1>(v)); v = fmaxf(v, dpp<XOR2>(v)); v = fmaxf(v, dpp<XOR7>(v)); v = fmaxf(v, dpp<XOR8>(v)); return v; }
__device__ __forceinline__ float xrow_sum(float x) {
  auto s = __builtin_amdgcn_permlane16_swap(f2u(x), f2u(x), false, false);
  x = u2f(s[0]) + u2f(s[1]);
  auto t = __builtin_amdgcn_permlane32_swap(f2u(x), f2u(x), false, false);
  return u2f(t[0]) + u2f(t[1]);
}
__device__ __forceinline__ float xrow_max(float x) {
  auto s = __builtin_amdgcn_permlane16_swap(f2u(x), f2u(x), false, false);
  x = fmaxf(u2f(s[0]), u2f(s[1]));
  auto t = __builtin_amdgcn_permlane32_swap(f2u(x), f2u(x), false, false);
  return fmaxf(u2f(t[0]), u2f(t[1]));
}
__device__ __forceinline__ float wave_sum(float v) { return xrow_sum(row_sum(v)); }
__device__ __forceinline__ rsrc_t mkrs(const void* p, unsigned bytes) { return __builtin_amdgcn_make_buffer_rsrc((void*)p, 0, bytes, 0x00020000); }
template <int AUX> __device__ __forceinline__ u32x4 ldx(rsrc_t rs, int voff, int soff) { return __builtin_amdgcn_raw_buffer_load_b128(rs, voff, soff, AUX); }
#define ldnt ldx<AUX_W>
__device__ __forceinline__ u32x4 ldsc(rsrc_t rs, int voff) { return __builtin_amdgcn_raw_buffer_load_b128(rs, voff, 0, AUX_SC1); }
__device__ __forceinline__ u32x2 ldsc8(rsrc_t rs, int voff) { return __builtin_amdgcn_raw_buffer_load_b64(rs, voff, 0, AUX_SC1); }
__device__ __forceinline__ void stsc(rsrc_t rs, int voff, u32x4 v) { __builtin_amdgcn_raw_buffer_store_b128(v, rs, voff, 0, AUX_SC1); }
__device__ __forceinline__ void stsc8(rsrc_t rs, int voff, u32x2 v) { __builtin_amdgcn_raw_buffer_store_b64(v, rs, voff, 0, AUX_SC1); }
template <int N> __device__ __forceinline__ void waitvm() { asm volatile("s_waitcnt vmcnt(%0)" ::"i"(N) : "memory"); }
template <int N> __device__ __forceinline__ void pace(int w) {
  asm volatile("s_cmp_eq_u32 %0, 0\n\ts_cbranch_scc1 1f\n\ts_waitcnt vmcnt(%1)\n1:" ::"s"(w), "i"(N) : "scc", "memory");
}
#define SCHED() __builtin_amdgcn_sched_barrier(0)
#define MEMBAR() asm volatile("" ::: "memory")
__device__ __forceinline__ void wg_barrier() { asm volatile("s_waitcnt lgkmcnt(0)\n\ts_barrier" ::: "memory"); }
__device__ __forceinline__ f32x4 mfma32(u32x4 a, u32x4 b, f32x4 c) {
  return __builtin_amdgcn_mfma_f32_16x16x32_bf16(__builtin_bit_cast(bf16x8, a), __builtin_bit_cast(bf16x8, b), c, 0, 0, 0);
}
__device__ __forceinline__ u32x2 trread(LAS unsigned char* p) { return __builtin_bit_cast(u32x2, __builtin_amdgcn_ds_read_tr16_b64_v4i16((LAS s16x4*)p)); }
__device__ __forceinline__ u32x4 trfrag(LAS unsigned char* p, int step) {
  const u32x2 lo = trread(p), hi = trread(p + step);
  return u32x4{lo.x, lo.y, hi.x, hi.y};
}
__device__ __forceinline__ float sel4(f32x4 v, int g) { const float a = (g & 1) ? v[1] : v[0], b = (g & 1) ? v[3] : v[2]; return (g & 2) ? b : a; }
__device__ __forceinline__ unsigned tag2(u32x4 v, unsigned tag) { return (v.y ^ tag) | (v.w ^ tag); }
__device__ __forceinline__ bool all0(unsigned bad) { return __builtin_amdgcn_ballot_w64(bad != 0u) == 0ull; }

#define STAMP(n) do { if ((n) == 0) pt0 = __builtin_amdgcn_s_memrealtime(); if ((n) == PROBE) pt1 = __builtin_amdgcn_s_memrealtime(); } while (0)

__global__ void __launch_bounds__(512, 2)
fused_block(const u16* __restrict__ xin, const u16* __restrict__ kc, const u16* __restrict__ vc, const u16* __restrict__ wq,
            const u16* __restrict__ wo, const u16* __restrict__ wg, const u16* __restrict__ wu, const u16* __restrict__ wd,
            const u16* __restrict__ cin, const u16* __restrict__ wmod, u16* __restrict__ out, unsigned char* ws) {
  __shared__ __attribute__((aligned(16))) unsigned char smem[L_BYTES];
  LAS unsigned char* L = (LAS unsigned char*)smem;
  volatile LAS unsigned* MISC = (volatile LAS unsigned*)(L + L_MISC);
  const int tid = threadIdx.x, lane = tid & 63, w = __builtin_amdgcn_readfirstlane(tid >> 6), g = lane >> 4, c = lane & 15;
  const int bid = blockIdx.x, x = bid & 7, slot = bid >> 3, h = 4 * x + (slot >> 3), j8 = slot & 7, b = j8 >> 1, s = j8 & 1;
  const bool isMod = slot < 15, isHU = isMod && x < 4 && slot < 10, isRed = slot < 20, isMlp = slot >= 20;
  const int hu = 10 * x + slot;
  const int ru = x < 4 ? 5 * x + (slot - 10) : 20 + 15 * (x - 4) + slot;
  const int rid = x * 20 + slot, rb = rid / 40, rt = rid % 40;
  const int mid = x * 12 + (slot - 20);
  unsigned long long pt0 = 0, pt1 = 0;
  STAMP(0);

  const rsrc_t rws = mkrs(ws, WS_END);
  const size_t kvbase = ((size_t)(b * NH + h) * SK + (SK - WIN) + s * 512 + w * 64) * DH;
  const rsrc_t rk = mkrs(kc + kvbase, 64 * 384), rv = mkrs(vc + kvbase, 64 * 384);
  const int kvo = g * 768 + c * 16;
  u32x4 K[24], V[24];
#define LDK(n) K[n] = ldx<AUX_K>(rk, kvo + ((n) % 3) * 256, ((n) / 3) * 3072)
#define LDV(n) V[n] = ldx<AUX_V>(rv, kvo + ((n) % 3) * 256, ((n) / 3) * 3072)

  u32x4 SR[8], SQ[4], SO[4];
  if (isMod) {
    const rsrc_t rm = mkrs(wmod, D * 6 * D * 2);
    const int c0 = isHU ? 16 * hu : 1280 + 32 * ru, c1 = isHU ? 640 + 16 * hu : 1296 + 32 * ru;
    const int ch = tid & 3, vo = (tid >> 2) * 7680 + ((ch & 2) ? c1 : c0) * 2 + (ch & 1) * 16;
#pragma unroll
    for (int k = 0; k < 5; ++k) SR[k] = ldnt(rm, vo, k * 128 * 7680);
  } else if (isMlp) {
    const rsrc_t rg = mkrs(w < 4 ? wg : wu, D * FF * 2), rd = mkrs(wd + (size_t)mid * 16 * D, 16 * D * 2);
    const int t2 = tid & 255, vo = (t2 >> 1) * 3072 + mid * 32 + (t2 & 1) * 16;
#pragma unroll
    for (int k = 0; k < 5; ++k) SR[k] = ldnt(rg, vo, k * 128 * 3072);
#pragma unroll
    for (int k = 0; k < 3; ++k) SR[5 + k] = ldnt(rd, tid * 16 + k * 8192, 0);
  }
  {
    const rsrc_t rq = mkrs(wq + (size_t)(160 * b) * (NH * DH) + DH * h + 96 * s, 160 * NH * DH * 2 - (DH * h + 96 * s) * 2);
    const rsrc_t ro = mkrs(wo + (size_t)(DH * h) * D + 80 * j8, DH * D * 2 - 80 * j8 * 2);
#pragma unroll
    for (int k = 0; k < 4; ++k) { const int u = tid + 512 * k, row = u / 12, ch = u % 12; SQ[k] = ldnt(rq, row * 12288 + ch * 16, 0); }
#pragma unroll
    for (int k = 0; k < 4; ++k) { const int u = tid + 512 * k, row = u / 10, ch = u % 10; SO[k] = ldnt(ro, row * 1280 + ch * 16, 0); }
  }
  SCHED();
  unsigned epoch = 0;
  if (w == 0) epoch = __hip_atomic_load((GAS unsigned*)(ws + WS_EPOCH), __ATOMIC_RELAXED, __HIP_MEMORY_SCOPE_AGENT);
  u32x4 cv = {0, 0, 0, 0};
  unsigned xr[5] = {0, 0, 0, 0, 0};
  unsigned xh = 0;
  if (isMod && tid < 320) cv = *(const u32x4*)(cin + tid * 8);
  if (isHU) {
    if (w >= 4) {
#pragma unroll
      for (int k = 0; k < 5; ++k) xr[k] = ((const unsigned*)xin)[(w - 4) * 320 + lane + 64 * k];
    }
    if (w == 0) xh = ((const unsigned*)xin)[g * 320 + 8 * hu + (c >> 1)];
  }
  unsigned xres = 0;
  if (isRed && tid < 8) xres = ((const unsigned*)xin)[rb * 320 + rt * 8 + tid];
  SCHED();
  auto prologue_finish = [&]() {
    if (tid == 0) { MISC[0] = (epoch + 1u) * 0x9E3779B1u; MISC[1] = 0u; MISC[2] = 0u; }
    if (isMod && tid < 320) {
      u32x4 o;
#pragma unroll
      for (int k = 0; k < 4; ++k) { const float a = bflo(cv[k]), bb = bfhi(cv[k]); o[k] = pack2(a / (1.f + __expf(-a)), bb / (1.f + __expf(-bb))); }
      *(LAS u32x4*)(L + L_A + (tid / 80) * 1344 + (tid % 80) * 16) = o;
    }
    if (isHU && w >= 4) {
      float sm = 0.f;
#pragma unroll
      for (int k = 0; k < 5; ++k) sm += bflo(xr[k]) + bfhi(xr[k]);
      const float mean = wave_sum(sm) * (1.f / D);
      float sv = 0.f;
#pragma unroll
      for (int k = 0; k < 5; ++k) { const float a = bflo(xr[k]) - mean, bb = bfhi(xr[k]) - mean; sv += a * a + bb * bb; }
      const float var = wave_sum(sv) * (1.f / D);
      if (lane == 0) { MISC[4 + 2 * (w - 4)] = f2u(rbf(mean)); MISC[5 + 2 * (w - 4)] = f2u(rbf(sqrtf(rbf(rbf(var) + 1e-5f)))); }
    }
    if (isMod) {
#pragma unroll
      for (int k = 0; k < 5; ++k) { const int row = (tid >> 2) + 128 * k; *(LAS u32x4*)(L + L_RL + row * 64 + (row >> 3) * 32 + (tid & 3) * 16) = SR[k]; }
    } else if (isMlp) {
      const int t2 = tid & 255;
#pragma unroll
      for (int k = 0; k < 5; ++k) { const int row = (t2 >> 1) + 128 * k; *(LAS u32x4*)(L + L_RL + row * 64 + (row >> 3) * 32 + (w >> 2) * 32 + (t2 & 1) * 16) = SR[k]; }
#pragma unroll
      for (int k = 0; k < 3; ++k) { const int u = tid + 512 * k; if (u < 1280) *(LAS u32x4*)(L + L_WD + (u / 80) * 1312 + (u % 80) * 16) = SR[5 + k]; }
    }
#pragma unroll
    for (int k = 0; k < 4; ++k) { const int u = tid + 512 * k, row = u / 12, ch = u % 12; if (u < 1920) *(LAS u32x4*)(L + L_WQ + row * 192 + (row >> 3) * 32 + ch * 16) = SQ[k]; }
#pragma unroll
    for (int k = 0; k < 4; ++k) { const int u = tid + 512 * k, row = u / 10, ch = u % 10; if (u < 1920) *(LAS u32x4*)(L + L_WO + row * 160 + (row >> 3) * 128 + ch * 16) = SO[k]; }
  };
  const int trq = (c >> 2), trp = (c & 3);

#pragma unroll
  for (int n = 0; n < W_WIN; ++n) LDK(n);
  SCHED();
  prologue_finish();
  wg_barrier();
  STAMP(1);
  const unsigned tag = MISC[0];
#pragma unroll
  for (int n = W_WIN; n < 24; ++n) { pace<W_WIN - 1>(w); LDK(n); SCHED(); }
  if (w == 0) {
    if (isMod) {
      f32x4 a0 = {0, 0, 0, 0}, a1 = {0, 0, 0, 0};
      LAS unsigned char* pa = L + L_A + (c & 3) * 1344 + g * 16;
      LAS unsigned char* pb = L + L_RL + g * 544 + trq * 64 + trp * 8;
#pragma unroll
      for (int ks = 0; ks < 20; ++ks) {
        const u32x4 af = *(LAS u32x4*)(pa + ks * 64);
        a0 = mfma32(af, trfrag(pb + ks * 2176, 256), a0);
        a1 = mfma32(af, trfrag(pb + ks * 2176 + 32, 256), a1);
      }
      const float v0 = rbf(sel4(a0, g)), v1 = rbf(sel4(a1, g));
      if (isHU) {
        const float mb = u2f(MISC[4 + 2 * g]), den = u2f(MISC[5 + 2 * g]);
        const float hv = rbf(rbf(rbf(rbf(((c & 1) ? bfhi(xh) : bflo(xh)) - mb) / den) * rbf(1.f + v1)) + v0);
        const float hn = dpp<XOR1>(hv);
        if ((c & 1) == 0) stsc8(rws, WS_HG + (g * 320 + 8 * hu + (c >> 1)) * 8, u32x2{pack2(hv, hn), tag});
      } else {
        const float n0 = dpp<XOR1>(v0), n1 = dpp<XOR1>(v1);
        const unsigned pv = (c & 1) ? pack2(n1, v1) : pack2(v0, n0);
        stsc8(rws, WS_MG + (g * 1280 + 16 * ru + (c & 1) * 8 + (c >> 1)) * 8, u32x2{pv, tag});
      }
    }
    STAMP(2);
    {
      u32x4 t[3];
      int off[3];
#pragma unroll
      for (int k = 0; k < 3; ++k) { const int n = min(lane + 64 * k, 159); off[k] = WS_HG + (n / 40) * 2560 + b * 640 + (n % 40) * 16; }
#pragma unroll 1
      for (unsigned spins = 0; spins < SPIN_CAP; ++spins) {
        MEMBAR();
#pragma unroll
        for (int k = 0; k < 3; ++k) t[k] = ldsc(rws, off[k]);
        if (all0(tag2(t[0], tag) | tag2(t[1], tag) | tag2(t[2], tag))) break;
        __builtin_amdgcn_s_sleep(4);
      }
#pragma unroll
      for (int k = 0; k < 3; ++k) { const int n = min(lane + 64 * k, 159); *(LAS u32x2*)(L + L_HA + (n / 40) * 320 + (n % 40) * 8) = u32x2{t[k].x, t[k].z}; }
    }
    STAMP(3);
    {
      f32x4 acc[6];
#pragma unroll
      for (int t = 0; t < 6; ++t) acc[t] = f32x4{0, 0, 0, 0};
      LAS unsigned char* pa = L + L_HA + (c & 3) * 320 + g * 16;
      LAS unsigned char* pb = L + L_WQ + g * 1568 + trq * 192 + trp * 8;
#pragma unroll
      for (int ks = 0; ks < 5; ++ks) {
        const u32x4 af = *(LAS u32x4*)(pa + ks * 64);
#pragma unroll
        for (int t = 0; t < 6; ++t) acc[t] = mfma32(af, trfrag(pb + ks * 6272 + t * 32, 768), acc[t]);
      }
#pragma unroll
      for (int tp = 0; tp < 3; ++tp)
        stsc(rws, WS_QG + ((((h * 4 + g) * 4 + b) * 2 + s) * 48 + tp * 16 + c) * 16, u32x4{f2u(sel4(acc[2 * tp], g)), tag, f2u(sel4(acc[2 * tp + 1], g)), tag});
    }
    STAMP(4);
    {
      u32x4 t[6];
      const int qb = WS_QG + (h * 4 + b) * 384 * 16;
      const int o1 = qb + lane * 16, o2 = qb + ((lane >> 5) * 192 + 64 + (lane & 31)) * 16;
#pragma unroll 1
      for (unsigned spins = 0; spins < SPIN_CAP; ++spins) {
        MEMBAR();
#pragma unroll
        for (int k = 0; k < 4; ++k) t[k] = ldsc(rws, o1 + k * 1536);
#pragma unroll
        for (int k = 0; k < 2; ++k) t[4 + k] = ldsc(rws, o2 + k * 1536);
        unsigned bad = 0;
#pragma unroll
        for (int k = 0; k < 6; ++k) bad |= tag2(t[k], tag);
        if (all0(bad)) break;
        __builtin_amdgcn_s_sleep(4);
      }
      const float qa = (u2f(t[0].x) + u2f(t[1].x)) + (u2f(t[2].x) + u2f(t[3].x)), qb2 = (u2f(t[0].z) + u2f(t[1].z)) + (u2f(t[2].z) + u2f(t[3].z));
      float qc = u2f(t[4].x) + u2f(t[5].x), qd = u2f(t[4].z) + u2f(t[5].z);
      { auto r = __builtin_amdgcn_permlane32_swap(f2u(qc), f2u(qc), false, false); qc = u2f(r[0]) + u2f(r[1]); }
      { auto r = __builtin_amdgcn_permlane32_swap(f2u(qd), f2u(qd), false, false); qd = u2f(r[0]) + u2f(r[1]); }
      LAS u16* Q = (LAS u16*)(L + L_Q);
      const int d1 = lane < 48 ? 32 * g + c : 96 + c;
      Q[d1] = (u16)pack2(qa, 0.f); Q[d1 + 16] = (u16)pack2(qb2, 0.f);
      const int d2 = 128 + 32 * (g & 1) + c;
      if (lane < 32) { Q[d2] = (u16)pack2(qc, 0.f); Q[d2 + 16] = (u16)pack2(qd, 0.f); }
      asm volatile("s_waitcnt lgkmcnt(0)" ::: "memory");
      MISC[1] = 1u;
    }
  }
  SCHED();
#pragma unroll
  for (int n = 0; n < NV0; ++n) { pace<W_WIN - 1>(w); LDV(n); SCHED(); }
  STAMP(5);
#pragma unroll 1
  for (unsigned spins = 0; MISC[1] == 0u && spins < (SPIN_CAP << 4); ++spins) __builtin_amdgcn_s_sleep(2);
  STAMP(13);
  SCHED();
#pragma unroll
  for (int n = NV0; n < 24; ++n) LDV(n);
  SCHED();

  unsigned pl;
  {
    LAS unsigned char* pq = L + L_Q;
    const u32x4 q0 = *(LAS u32x4*)(pq + c * 16), q1 = *(LAS u32x4*)(pq + ((16 + c) % 24) * 16), q2 = *(LAS u32x4*)(pq + (8 + c) * 16);
    float v[16];
#pragma unroll
    for (int a = 0; a < 8; ++a) {
      float d1 = 0.f;
#pragma unroll
      for (int k = 0; k < 4; ++k) d1 = dot2(K[3 * a + 1][k], q1[k], d1);
      float e = c < 8 ? d1 : 0.f, o = c < 8 ? 0.f : d1;
#pragma unroll
      for (int k = 0; k < 4; ++k) { e = dot2(K[3 * a][k], q0[k], e); o = dot2(K[3 * a + 2][k], q2[k], o); }
      v[2 * a] = e; v[2 * a + 1] = o;
    }
#define TR_STEP(HALF, CTRL) _Pragma("unroll") for (int k = 0; k < (HALF); ++k) { \
      const float t1 = v[k] + dpp<CTRL>(v[k]), t2 = v[k + (HALF)] + dpp<CTRL>(v[k + (HALF)]); v[k] = (c & (HALF)) ? t2 : t1; }
    TR_STEP(8, XOR8) TR_STEP(4, XOR7) TR_STEP(2, XOR2) TR_STEP(1, XOR1)
#undef TR_STEP
    const float sc = rbf(rbf(v[0]) * 0.07216878364870322f);
    const float mw = xrow_max(row_max(sc));
    const float p = __expf(sc - mw);
    const float lw = wave_sum(p);
    pl = pack2(p, 0.f);
    if (lane == 0) { MISC[16 + w] = f2u(mw); MISC[24 + w] = f2u(lw); }
    asm volatile("s_waitcnt lgkmcnt(0)" ::: "memory");
    unsigned old = 0;
    if (lane == 0) old = __hip_atomic_fetch_add((LAS unsigned*)(L + L_MISC + 8), 1u, __ATOMIC_RELAXED, __HIP_MEMORY_SCOPE_WORKGROUP);
    if (__builtin_amdgcn_readfirstlane(old) == 7u) {
      const float m = u2f(MISC[16 + (c & 7)]), l = u2f(MISC[24 + (c & 7)]);
      const float M = row_max(m);
      const float Ls = row_sum(l * __expf(m - M)) * 0.5f;
      if (lane == 0) stsc(rws, WS_ML + (h * 8 + j8) * 16, u32x4{f2u(M), tag, f2u(Ls), tag});
    }
  }
  STAMP(6);
  SCHED();
  u32x4 BO[6];
  if (w < 5) {
    LAS unsigned char* pb = L + L_WO + g * 1408 + trq * 160 + trp * 8 + w * 32;
#pragma unroll
    for (int ks = 0; ks < 6; ++ks) BO[ks] = trfrag(pb + ks * 5632, 640);
  }
  float acc[24];
#pragma unroll
  for (int k = 0; k < 24; ++k) acc[k] = 0.f;
  const unsigned ph = pl << 16;
  float fsc = 0.f;
#define PV(a) { \
    const unsigned el = dppu<0x150 + 2 * (a)>(pl), ol = dppu<0x151 + 2 * (a)>(pl), eh = dppu<0x150 + 2 * (a)>(ph), oh = dppu<0x151 + 2 * (a)>(ph); \
    const unsigned ml = c < 8 ? el : ol, mh = c < 8 ? eh : oh; \
    _Pragma("unroll") for (int k = 0; k < 4; ++k) { \
      acc[2 * k] = dot2(V[3 * (a)][k], el, acc[2 * k]); acc[2 * k + 1] = dot2(V[3 * (a)][k], eh, acc[2 * k + 1]); \
      acc[8 + 2 * k] = dot2(V[3 * (a) + 1][k], ml, acc[8 + 2 * k]); acc[9 + 2 * k] = dot2(V[3 * (a) + 1][k], mh, acc[9 + 2 * k]); \
      acc[16 + 2 * k] = dot2(V[3 * (a) + 2][k], ol, acc[16 + 2 * k]); acc[17 + 2 * k] = dot2(V[3 * (a) + 2][k], oh, acc[17 + 2 * k]); } \
    SCHED(); }
  PV(0) PV(1) PV(2) PV(3) PV(4) PV(5)
  {
#pragma unroll 1
    for (unsigned spins = 0; MISC[2] < 8u && spins < (SPIN_CAP << 4); ++spins) __builtin_amdgcn_s_sleep(1);
    const float m = u2f(MISC[16 + (c >> 1)]);
    fsc = __expf(m - row_max(m));
  }
  SCHED();
  PV(6) PV(7)
#undef PV
  STAMP(7);
  {
    float u[12], z[6];
#pragma unroll
    for (int k = 0; k < 12; ++k) { auto r = __builtin_amdgcn_permlane32_swap(f2u(acc[k]), f2u(acc[k + 12]), false, false); u[k] = u2f(r[0]) + u2f(r[1]); }
#pragma unroll
    for (int k = 0; k < 6; ++k) { auto r = __builtin_amdgcn_permlane16_swap(f2u(u[k]), f2u(u[k + 6]), false, false); z[k] = u2f(r[0]) + u2f(r[1]); }
    LAS float* op = (LAS float*)(L + L_OP + w * 1552 + g * 384 + c * 4);
#pragma unroll
    for (int k = 0; k < 6; ++k) op[k * 16] = z[k];
  }
  wg_barrier();
  {
    float ov[6];
#pragma unroll
    for (int k = 0; k < 6; ++k) {
      const int d = 24 * w + 6 * g + k, ck = 24 * (c & 1) + (d >> 3);
      ov[k] = *(LAS float*)(L + L_OP + (c >> 1) * 1552 + ((ck >> 4) * 8 + (d & 7)) * 64 + (ck & 15) * 4) * fsc;
    }
#pragma unroll
    for (int k = 0; k < 6; ++k) ov[k] = row_sum(ov[k]);
    const float lo = c == 0 ? ov[0] : c == 1 ? ov[2] : ov[4], hi = c == 0 ? ov[1] : c == 1 ? ov[3] : ov[5];
    if (c < 3) stsc(rws, WS_OG + ((h * 8 + j8) * 96 + 12 * w + 3 * g + c) * 16, u32x4{f2u(lo), tag, f2u(hi), tag});
  }
  STAMP(8);
  if (w < 6) {
    const int ob = tid / 96, ou = tid % 96;
    const int o0 = WS_OG + ((h * 8 + 2 * ob) * 96 + ou) * 16, m0 = WS_ML + (h * 8 + 2 * ob) * 16;
    u32x4 t0, t1, ma, mb;
#pragma unroll 1
    for (unsigned spins = 0; spins < SPIN_CAP; ++spins) {
      MEMBAR();
      t0 = ldsc(rws, o0); t1 = ldsc(rws, o0 + 1536); ma = ldsc(rws, m0); mb = ldsc(rws, m0 + 16);
      if (all0(tag2(t0, tag) | tag2(t1, tag) | tag2(ma, tag) | tag2(mb, tag))) break;
      __builtin_amdgcn_s_sleep(1);
    }
    const float M = fmaxf(u2f(ma.x), u2f(mb.x)), e0 = __expf(u2f(ma.x) - M), e1 = __expf(u2f(mb.x) - M);
    const float inv = 1.f / (u2f(ma.z) * e0 + u2f(mb.z) * e1), w0 = e0 * inv, w1 = e1 * inv;
    *(LAS unsigned*)(L + L_OA + ob * 448 + ou * 4) = pack2(u2f(t0.x) * w0 + u2f(t1.x) * w1, u2f(t0.z) * w0 + u2f(t1.z) * w1);
  }
  wg_barrier();
  STAMP(9);
  if (w < 5) {
    f32x4 y = {0, 0, 0, 0};
    LAS unsigned char* pa = L + L_OA + (c & 3) * 448 + g * 16;
#pragma unroll
    for (int ks = 0; ks < 6; ++ks) y = mfma32(*(LAS u32x4*)(pa + ks * 64), BO[ks], y);
    stsc8(rws, WS_YG + ((g * 40 + 5 * j8 + w) * 32 + h) * 128 + c * 8, u32x2{f2u(sel4(y, g)), tag});
  }
  STAMP(10);
  if (isRed) {
    float x1a = 0.f, x1b = 0.f; unsigned gb2 = 0;
    if (w == 0) {
      u32x4 t[4]; u32x2 ga, gb;
      const int yo = WS_YG + (rb * 40 + rt) * 4096 + lane * 16, mo = WS_MG + (rb * 1280 + 8 * rt + (lane & 7)) * 8;
#pragma unroll 1
      for (unsigned spins = 0; spins < SPIN_CAP; ++spins) {
        MEMBAR();
#pragma unroll
        for (int k = 0; k < 4; ++k) t[k] = ldsc(rws, yo + k * 1024);
        ga = ldsc8(rws, mo); gb = ldsc8(rws, mo + 960 * 8);
        if (all0(tag2(t[0], tag) | tag2(t[1], tag) | tag2(t[2], tag) | tag2(t[3], tag) | (ga.y ^ tag) | (gb.y ^ tag))) break;
        __builtin_amdgcn_s_sleep(1);
      }
      float ya = (u2f(t[0].x) + u2f(t[1].x)) + (u2f(t[2].x) + u2f(t[3].x)), yb = (u2f(t[0].z) + u2f(t[1].z)) + (u2f(t[2].z) + u2f(t[3].z));
      ya += dpp<XOR8>(ya); yb += dpp<XOR8>(yb);
      ya = xrow_sum(ya); yb = xrow_sum(yb);
      x1a = rbf(bflo(xres) + rbf(bflo(ga.x) * rbf(ya))); x1b = rbf(bfhi(xres) + rbf(bfhi(ga.x) * rbf(yb)));
      gb2 = gb.x;
      if (lane < 8) stsc8(rws, WS_XG + (rb * 320 + 8 * rt + lane) * 8, u32x2{pack2(x1a, x1b), tag});
    }
    STAMP(11);
    if (w < 6) {
      u32x4 t0, t1;
      const int po = WS_PG + (rb * 40 + rt) * 12288 + tid * 16;
#pragma unroll 1
      for (unsigned spins = 0; spins < SPIN_CAP; ++spins) {
        MEMBAR();
        t0 = ldsc(rws, po); t1 = ldsc(rws, po + 6144);
        if (all0(tag2(t0, tag) | tag2(t1, tag))) break;
        __builtin_amdgcn_s_sleep(2);
      }
      float ya = u2f(t0.x) + u2f(t1.x), yb = u2f(t0.z) + u2f(t1.z);
      ya += dpp<XOR8>(ya); yb += dpp<XOR8>(yb);
      ya = xrow_sum(ya); yb = xrow_sum(yb);
      if (lane < 8) *(LAS u32x2*)(L + L_RED + w * 64 + lane * 8) = u32x2{f2u(ya), f2u(yb)};
    }
    wg_barrier();
    if (tid < 8) {
      float ya = 0.f, yb = 0.f;
#pragma unroll
      for (int k = 0; k < 6; ++k) { const u32x2 r = *(LAS u32x2*)(L + L_RED + k * 64 + lane * 8); ya += u2f(r.x); yb += u2f(r.y); }
      ((unsigned*)out)[rb * 320 + 8 * rt + lane] = pack2(x1a + rbf(bflo(gb2) * rbf(ya)), x1b + rbf(bfhi(gb2) * rbf(yb)));
      if (bid == 0 && tid == 0) __hip_atomic_store((GAS unsigned*)(ws + WS_EPOCH), epoch + 1u, __ATOMIC_RELAXED, __HIP_MEMORY_SCOPE_AGENT);
    }
    STAMP(12);
    if (PROBE > 0 && bid == 0 && w == 0) { const unsigned long long te = __builtin_amdgcn_s_memrealtime() + 2 * (pt1 - pt0); while (__builtin_amdgcn_s_memrealtime() < te) __builtin_amdgcn_s_sleep(1); }
  } else {
    u32x4 BG[5]; u32x2 BD[5];
    {
      LAS unsigned char* pb = L + L_RL + (w >> 1) * (5 * 2176) + g * 544 + trq * 64 + trp * 8 + (w & 1) * 32;
#pragma unroll
      for (int ks = 0; ks < 5; ++ks) BG[ks] = trfrag(pb + ks * 2176, 256);
      LAS unsigned char* pd = L + L_WD + (4 * g + trq) * 1312 + trp * 8 + w * 160;
#pragma unroll
      for (int t = 0; t < 5; ++t) BD[t] = trread(pd + t * 32);
    }
    if (w < 4) {
      u32x4 sb[3], cb[3], t[3];
      int oo[3];
#pragma unroll
      for (int k = 0; k < 3; ++k) oo[k] = min(lane + 64 * k, 159) * 16;
#pragma unroll 1
      for (unsigned spins = 0; spins < SPIN_CAP; ++spins) {
        MEMBAR();
        unsigned bad = 0;
#pragma unroll
        for (int k = 0; k < 3; ++k) { sb[k] = ldsc(rws, WS_MG + w * 10240 + 2560 + oo[k]); cb[k] = ldsc(rws, WS_MG + w * 10240 + 5120 + oo[k]); }
#pragma unroll
        for (int k = 0; k < 3; ++k) bad |= tag2(sb[k], tag) | tag2(cb[k], tag);
        if (all0(bad)) break;
        __builtin_amdgcn_s_sleep(2);
      }
#pragma unroll 1
      for (unsigned spins = 0; spins < SPIN_CAP; ++spins) {
        MEMBAR();
#pragma unroll
        for (int k = 0; k < 3; ++k) t[k] = ldsc(rws, WS_XG + w * 2560 + oo[k]);
        if (all0(tag2(t[0], tag) | tag2(t[1], tag) | tag2(t[2], tag))) break;
        __builtin_amdgcn_s_sleep(1);
      }
      STAMP(11);
      float xv[12];
#pragma unroll
      for (int k = 0; k < 3; ++k) { xv[4 * k] = bflo(t[k].x); xv[4 * k + 1] = bfhi(t[k].x); xv[4 * k + 2] = bflo(t[k].z); xv[4 * k + 3] = bfhi(t[k].z); }
      const bool l3 = lane < 32;
      float sm = ((xv[0] + xv[1]) + (xv[2] + xv[3])) + ((xv[4] + xv[5]) + (xv[6] + xv[7]));
      sm += l3 ? (xv[8] + xv[9]) + (xv[10] + xv[11]) : 0.f;
      const float mean = wave_sum(sm) * (1.f / D);
      float sv = 0.f, sv3 = 0.f;
#pragma unroll
      for (int k = 0; k < 8; ++k) { const float dlt = xv[k] - mean; sv = fmaf(dlt, dlt, sv); }
#pragma unroll
      for (int k = 8; k < 12; ++k) { const float dlt = xv[k] - mean; sv3 = fmaf(dlt, dlt, sv3); }
      sv += l3 ? sv3 : 0.f;
      const float var = wave_sum(sv) * (1.f / D);
      const float mb = rbf(mean), den = rbf(sqrtf(rbf(rbf(var) + 1e-5f)));
#pragma unroll
      for (int k = 0; k < 3; ++k) {
        const float s0 = bflo(sb[k].x), s1 = bfhi(sb[k].x), s2 = bflo(sb[k].z), s3 = bfhi(sb[k].z);
        const float c0 = bflo(cb[k].x), c1 = bfhi(cb[k].x), c2 = bflo(cb[k].z), c3 = bfhi(cb[k].z);
        const float h0 = rbf(rbf(rbf(xv[4 * k] - mb) / den) * rbf(1.f + c0)) + s0, h1 = rbf(rbf(rbf(xv[4 * k + 1] - mb) / den) * rbf(1.f + c1)) + s1;
        const float h2 = rbf(rbf(rbf(xv[4 * k + 2] - mb) / den) * rbf(1.f + c2)) + s2, h3 = rbf(rbf(rbf(xv[4 * k + 3] - mb) / den) * rbf(1.f + c3)) + s3;
        *(LAS u32x2*)(L + L_A + w * 1344 + (oo[k] >> 1)) = u32x2{pack2(h0, h1), pack2(h2, h3)};
      }
    }
    wg_barrier();
    {
      f32x4 a = {0, 0, 0, 0};
      LAS unsigned char* pa = L + L_A + (c & 3) * 1344 + (w >> 1) * 320 + g * 16;
#pragma unroll
      for (int ks = 0; ks < 5; ++ks) a = mfma32(*(LAS u32x4*)(pa + ks * 64), BG[ks], a);
      *(LAS float*)(L + L_RED + w * 256 + lane * 4) = sel4(a, g);
    }
    wg_barrier();
    {
      f32x4 gs = {0, 0, 0, 0}, us = {0, 0, 0, 0};
#pragma unroll
      for (int k = 0; k < 4; ++k) {
        gs += *(LAS f32x4*)(L + L_RED + (2 * k) * 256 + (c & 3) * 64 + g * 16);
        us += *(LAS f32x4*)(L + L_RED + (2 * k + 1) * 256 + (c & 3) * 64 + g * 16);
      }
      float hh[4];
#pragma unroll
      for (int k = 0; k < 4; ++k) { const float gv = rbf(gs[k]); hh[k] = gv * (gv > 0.f ? 1.f : 0.f) * rbf(us[k]); }
      const u32x2 af = {pack2(hh[0], hh[1]), pack2(hh[2], hh[3])};
#pragma unroll
      for (int t = 0; t < 5; ++t) {
        f32x4 y = {0, 0, 0, 0};
        y = __builtin_amdgcn_mfma_f32_16x16x16bf16_1k(__builtin_bit_cast(s16x4, af), __builtin_bit_cast(s16x4, BD[t]), y, 0, 0, 0);
        stsc8(rws, WS_PG + ((g * 40 + 5 * w + t) * 96 + mid) * 128 + c * 8, u32x2{f2u(sel4(y, g)), tag});
      }
    }
    STAMP(12);
  }
}

extern "C" void kernel_launch(void* const* d_in, const int* in_sizes, int n_in, void* d_out, int out_size, void* d_ws, size_t ws_size, hipStream_t stream) {
  fused_block<<<256, 512, 0, stream>>>((const u16*)d_in[0], (const u16*)d_in[1], (const u16*)d_in[2], (const u16*)d_in[3], (const u16*)d_in[4],
                                       (const u16*)d_in[6], (const u16*)d_in[7], (const u16*)d_in[8], (const u16*)d_in[9], (const u16*)d_in[10],
                                       (u16*)d_out, (unsigned char*)d_ws);
}
```

```cpp
#define PROBE 0
#include <hip/hip_runtime.h>
#include <hip/hip_bf16.h>
#include <stdint.h>

#define W_WIN 5
#ifndef NV0
#define NV0 9
#endif
#define AUX_K 2
#define AUX_V 2
#define AUX_W 2
#define STAMPS 0

typedef unsigned short u16;
typedef unsigned u32x4 __attribute__((ext_vector_type(4)));
typedef unsigned u32x2 __attribute__((ext_vector_type(2)));
typedef float f32x4 __attribute__((ext_vector_type(4)));
typedef short s16x4 __attribute__((ext_vector_type(4)));
typedef short s16x8 __attribute__((ext_vector_type(8)));
typedef __bf16 bf16x2 __attribute__((ext_vector_type(2)));
typedef __bf16 bf16x8 __attribute__((ext_vector_type(8)));
#define LAS __attribute__((address_space(3)))
#define GAS __attribute__((address_space(1)))
typedef __amdgpu_buffer_rsrc_t rsrc_t;

constexpr int D = 640, NH = 32, DH = 192, SK = 4096, WIN = 1024, FF = 1536;
constexpr int AUX_NT = 2, AUX_SC1 = 16;
constexpr unsigned SPIN_CAP = 1u << 16;

constexpr int WS_EPOCH = 0;
constexpr int WS_HG = 1024;
constexpr int WS_XG = WS_HG + 4 * 320 * 8;
constexpr int WS_MG = WS_XG + 4 * 320 * 8;
constexpr int WS_ML = WS_MG + 4 * 1280 * 8;
constexpr int WS_OG = WS_ML + 256 * 16;
constexpr int WS_QG = WS_OG + 256 * 96 * 16;
constexpr int WS_YG = WS_QG + 32 * 4 * 384 * 16;
constexpr int WS_PG = WS_YG + 4 * 40 * 32 * 128;
constexpr int WS_END = WS_PG + 4 * 40 * 96 * 128;
constexpr int WS_STAMP = 8 << 20;

constexpr int L_WQ = 0;
constexpr int L_WO = L_WQ + 160 * 192 + 20 * 32;
constexpr int L_RL = L_WO + 192 * 160 + 24 * 128;
constexpr int L_WD = L_RL + 80 * 544;
constexpr int L_A = L_WD + 16 * 1312;
constexpr int L_HA = L_A + 4 * 1344;
constexpr int L_OA = L_HA + 4 * 320;
constexpr int L_Q = L_OA + 4 * 448;
constexpr int L_OP = L_Q + 384;
constexpr int L_RED = L_OP + 8 * 1552;
constexpr int L_MISC = L_RED + 8 * 256;
constexpr int L_BYTES = L_MISC + 256;
static_assert(L_BYTES <= 160 * 1024 && L_BYTES > 80 * 1024, "one workgroup per CU");

__device__ __forceinline__ float bflo(unsigned u) { return __builtin_bit_cast(float, u << 16); }
__device__ __forceinline__ float bfhi(unsigned u) { return __builtin_bit_cast(float, u & 0xffff0000u); }
__device__ __forceinline__ float rbf(float f) { return (float)(__bf16)f; }
__device__ __forceinline__ unsigned pack2(float lo, float hi) { bf16x2 v = {(__bf16)lo, (__bf16)hi}; return __builtin_bit_cast(unsigned, v); }
__device__ __forceinline__ unsigned f2u(float f) { return __builtin_bit_cast(unsigned, f); }
__device__ __forceinline__ float u2f(unsigned u) { return __builtin_bit_cast(float, u); }
__device__ __forceinline__ float dot2(unsigned a, unsigned b, float c) {
  return __builtin_amdgcn_fdot2_f32_bf16(__builtin_bit_cast(bf16x2, a), __builtin_bit_cast(bf16x2, b), c, false);
}
template <int CTRL> __device__ __forceinline__ float dpp(float x) { return u2f((unsigned)__builtin_amdgcn_mov_dpp((int)f2u(x), CTRL, 0xf, 0xf, true)); }
template <int CTRL> __device__ __forceinline__ unsigned dppu(unsigned x) { return (unsigned)__builtin_amdgcn_mov_dpp((int)x, CTRL, 0xf, 0xf, true); }
constexpr int XOR1 = 0xB1, XOR2 = 0x4E, XOR7 = 0x141, XOR8 = 0x128, XOR15 = 0x140;
__device__ __forceinline__ float row_sum(float v) { v += dpp<XOR1>(v); v += dpp<XOR2>(v); v += dpp<XOR7>(v); v += dpp<XOR8>(v); return v; }
__device__ __forceinline__ float row_max(float v) { v = fmaxf(v, dpp<XOR1>(v)); v = fmaxf(v, dpp<XOR2>(v)); v = fmaxf(v, dpp<XOR7>(v)); v = fmaxf(v, dpp<XOR8>(v)); return v; }
__device__ __forceinline__ float xrow_sum(float x) {
  auto s = __builtin_amdgcn_permlane16_swap(f2u(x), f2u(x), false, false);
  x = u2f(s[0]) + u2f(s[1]);
  auto t = __builtin_amdgcn_permlane32_swap(f2u(x), f2u(x), false, false);
  return u2f(t[0]) + u2f(t[1]);
}
__device__ __forceinline__ float xrow_max(float x) {
  auto s = __builtin_amdgcn_permlane16_swap(f2u(x), f2u(x), false, false);
  x = fmaxf(u2f(s[0]), u2f(s[1]));
  auto t = __builtin_amdgcn_permlane32_swap(f2u(x), f2u(x), false, false);
  return fmaxf(u2f(t[0]), u2f(t[1]));
}
__device__ __forceinline__ float wave_sum(float v) { return xrow_sum(row_sum(v)); }
__device__ __forceinline__ rsrc_t mkrs(const void* p, unsigned bytes) { return __builtin_amdgcn_make_buffer_rsrc((void*)p, 0, bytes, 0x00020000); }
template <int AUX> __device__ __forceinline__ u32x4 ldx(rsrc_t rs, int voff, int soff) { return __builtin_amdgcn_raw_buffer_load_b128(rs, voff, soff, AUX); }
#define ldnt ldx<AUX_W>
__device__ __forceinline__ u32x4 ldsc(rsrc_t rs, int voff) { return __builtin_amdgcn_raw_buffer_load_b128(rs, voff, 0, AUX_SC1); }
__device__ __forceinline__ u32x2 ldsc8(rsrc_t rs, int voff) { return __builtin_amdgcn_raw_buffer_load_b64(rs, voff, 0, AUX_SC1); }
__device__ __forceinline__ void stsc(rsrc_t rs, int voff, u32x4 v) { __builtin_amdgcn_raw_buffer_store_b128(v, rs, voff, 0, AUX_SC1); }
__device__ __forceinline__ void stsc8(rsrc_t rs, int voff, u32x2 v) { __builtin_amdgcn_raw_buffer_store_b64(v, rs, voff, 0, AUX_SC1); }
template <int N> __device__ __forceinline__ void waitvm() { asm volatile("s_waitcnt vmcnt(%0)" ::"i"(N) : "memory"); }
template <int N> __device__ __forceinline__ void pace(int w) {
  asm volatile("s_cmp_eq_u32 %0, 0\n\ts_cbranch_scc1 1f\n\ts_waitcnt vmcnt(%1)\n1:" ::"s"(w), "i"(N) : "scc", "memory");
}
#define SCHED() __builtin_amdgcn_sched_barrier(0)
#define MEMBAR() asm volatile("" ::: "memory")
__device__ __forceinline__ void wg_barrier() { asm volatile("s_waitcnt lgkmcnt(0)\n\ts_barrier" ::: "memory"); }
__device__ __forceinline__ f32x4 mfma32(u32x4 a, u32x4 b, f32x4 c) {
  return __builtin_amdgcn_mfma_f32_16x16x32_bf16(__builtin_bit_cast(bf16x8, a), __builtin_bit_cast(bf16x8, b), c, 0, 0, 0);
}
__device__ __forceinline__ u32x2 trread(LAS unsigned char* p) { return __builtin_bit_cast(u32x2, __builtin_amdgcn_ds_read_tr16_b64_v4i16((LAS s16x4*)p)); }
__device__ __forceinline__ u32x4 trfrag(LAS unsigned char* p, int step) {
  const u32x2 lo = trread(p), hi = trread(p + step);
  return u32x4{lo.x, lo.y, hi.x, hi.y};
}
__device__ __forceinline__ float sel4(f32x4 v, int g) { const float a = (g & 1) ? v[1] : v[0], b = (g & 1) ? v[3] : v[2]; return (g & 2) ? b : a; }
__device__ __forceinline__ unsigned tag2(u32x4 v, unsigned tag) { return (v.y ^ tag) | (v.w ^ tag); }
__device__ __forceinline__ bool all0(unsigned bad) { return __builtin_amdgcn_ballot_w64(bad != 0u) == 0ull; }

#define STAMP(n) do {} while (0)

__global__ void __launch_bounds__(512, 2)
fused_block(const u16* __restrict__ xin, const u16* __restrict__ kc, const u16* __restrict__ vc, const u16* __restrict__ wq,
            const u16* __restrict__ wo, const u16* __restrict__ wg, const u16* __restrict__ wu, const u16* __restrict__ wd,
            const u16* __restrict__ cin, const u16* __restrict__ wmod, u16* __restrict__ out, unsigned char* ws) {
  __shared__ __attribute__((aligned(16))) unsigned char smem[L_BYTES];
  LAS unsigned char* L = (LAS unsigned char*)smem;
  volatile LAS unsigned* MISC = (volatile LAS unsigned*)(L + L_MISC);
  const int tid = threadIdx.x, lane = tid & 63, w = __builtin_amdgcn_readfirstlane(tid >> 6), g = lane >> 4, c = lane & 15;
  const int bid = blockIdx.x, x = bid & 7, slot = bid >> 3, h = 4 * x + (slot >> 3), j8 = slot & 7, b = j8 >> 1, s = j8 & 1;
  const bool isMod = slot < 15, isHU = isMod && x < 4 && slot < 10, isRed = slot < 20, isMlp = slot >= 20;
  const int hu = 10 * x + slot;
  const int ru = x < 4 ? 5 * x + (slot - 10) : 20 + 15 * (x - 4) + slot;
  const int rid = x * 20 + slot, rb = rid / 40, rt = rid % 40;
  const int mid = x * 12 + (slot - 20);
  const unsigned long long pt0 = __builtin_amdgcn_s_memrealtime();
  if (tid == 0) __hip_atomic_store((GAS unsigned long long*)(ws + WS_STAMP) + bid, pt0, __ATOMIC_RELAXED, __HIP_MEMORY_SCOPE_AGENT);

  const rsrc_t rws = mkrs(ws, WS_END);
  const size_t kvbase = ((size_t)(b * NH + h) * SK + (SK - WIN) + s * 512 + w * 64) * DH;
  const rsrc_t rk = mkrs(kc + kvbase, 64 * 384), rv = mkrs(vc + kvbase, 64 * 384);
  const int kvo = g * 768 + c * 16;
  u32x4 K[24], V[24];
#define LDK(n) K[n] = ldx<AUX_K>(rk, kvo + ((n) % 3) * 256, ((n) / 3) * 3072)
#define LDV(n) V[n] = ldx<AUX_V>(rv, kvo + ((n) % 3) * 256, ((n) / 3) * 3072)

  u32x4 SR[8], SQ[4], SO[4];
  if (isMod) {
    const rsrc_t rm = mkrs(wmod, D * 6 * D * 2);
    const int c0 = isHU ? 16 * hu : 1280 + 32 * ru, c1 = isHU ? 640 + 16 * hu : 1296 + 32 * ru;
    const int ch = tid & 3, vo = (tid >> 2) * 7680 + ((ch & 2) ? c1 : c0) * 2 + (ch & 1) * 16;
#pragma unroll
    for (int k = 0; k < 5; ++k) SR[k] = ldnt(rm, vo, k * 128 * 7680);
  } else if (isMlp) {
    const rsrc_t rg = mkrs(w < 4 ? wg : wu, D * FF * 2), rd = mkrs(wd + (size_t)mid * 16 * D, 16 * D * 2);
    const int t2 = tid & 255, vo = (t2 >> 1) * 3072 + mid * 32 + (t2 & 1) * 16;
#pragma unroll
    for (int k = 0; k < 5; ++k) SR[k] = ldnt(rg, vo, k * 128 * 3072);
#pragma unroll
    for (int k = 0; k < 3; ++k) SR[5 + k] = ldnt(rd, tid * 16 + k * 8192, 0);
  }
  {
    const rsrc_t rq = mkrs(wq + (size_t)(160 * b) * (NH * DH) + DH * h + 96 * s, 160 * NH * DH * 2 - (DH * h + 96 * s) * 2);
    const rsrc_t ro = mkrs(wo + (size_t)(DH * h) * D + 80 * j8, DH * D * 2 - 80 * j8 * 2);
#pragma unroll
    for (int k = 0; k < 4; ++k) { const int u = tid + 512 * k, row = u / 12, ch = u % 12; SQ[k] = ldnt(rq, row * 12288 + ch * 16, 0); }
#pragma unroll
    for (int k = 0; k < 4; ++k) { const int u = tid + 512 * k, row = u / 10, ch = u % 10; SO[k] = ldnt(ro, row * 1280 + ch * 16, 0); }
  }
  SCHED();
  unsigned epoch = 0;
  if (w == 0) epoch = __hip_atomic_load((GAS unsigned*)(ws + WS_EPOCH), __ATOMIC_RELAXED, __HIP_MEMORY_SCOPE_AGENT);
  u32x4 cv = {0, 0, 0, 0};
  unsigned xr[5] = {0, 0, 0, 0, 0};
  unsigned xh = 0;
  if (isMod && tid < 320) cv = *(const u32x4*)(cin + tid * 8);
  if (isHU) {
    if (w >= 4) {
#pragma unroll
      for (int k = 0; k < 5; ++k) xr[k] = ((const unsigned*)xin)[(w - 4) * 320 + lane + 64 * k];
    }
    if (w == 0) xh = ((const unsigned*)xin)[g * 320 + 8 * hu + (c >> 1)];
  }
  unsigned xres = 0;
  if (isRed && tid < 8) xres = ((const unsigned*)xin)[rb * 320 + rt * 8 + tid];
  SCHED();
  auto prologue_finish = [&]() {
    if (tid == 0) { MISC[0] = (epoch + 1u) * 0x9E3779B1u; MISC[1] = 0u; MISC[2] = 0u; }
    if (isMod && tid < 320) {
      u32x4 o;
#pragma unroll
      for (int k = 0; k < 4; ++k) { const float a = bflo(cv[k]), bb = bfhi(cv[k]); o[k] = pack2(a / (1.f + __expf(-a)), bb / (1.f + __expf(-bb))); }
      *(LAS u32x4*)(L + L_A + (tid / 80) * 1344 + (tid % 80) * 16) = o;
    }
    if (isHU && w >= 4) {
      float sm = 0.f;
#pragma unroll
      for (int k = 0; k < 5; ++k) sm += bflo(xr[k]) + bfhi(xr[k]);
      const float mean = wave_sum(sm) * (1.f / D);
      float sv = 0.f;
#pragma unroll
      for (int k = 0; k < 5; ++k) { const float a = bflo(xr[k]) - mean, bb = bfhi(xr[k]) - mean; sv += a * a + bb * bb; }
      const float var = wave_sum(sv) * (1.f / D);
      if (lane == 0) { MISC[4 + 2 * (w - 4)] = f2u(rbf(mean)); MISC[5 + 2 * (w - 4)] = f2u(rbf(sqrtf(rbf(rbf(var) + 1e-5f)))); }
    }
    if (isMod) {
#pragma unroll
      for (int k = 0; k < 5; ++k) { const int row = (tid >> 2) + 128 * k; *(LAS u32x4*)(L + L_RL + row * 64 + (row >> 3) * 32 + (tid & 3) * 16) = SR[k]; }
    } else if (isMlp) {
      const int t2 = tid & 255;
#pragma unroll
      for (int k = 0; k < 5; ++k) { const int row = (t2 >> 1) + 128 * k; *(LAS u32x4*)(L + L_RL + row * 64 + (row >> 3) * 32 + (w >> 2) * 32 + (t2 & 1) * 16) = SR[k]; }
#pragma unroll
      for (int k = 0; k < 3; ++k) { const int u = tid + 512 * k; if (u < 1280) *(LAS u32x4*)(L + L_WD + (u / 80) * 1312 + (u % 80) * 16) = SR[5 + k]; }
    }
#pragma unroll
    for (int k = 0; k < 4; ++k) { const int u = tid + 512 * k, row = u / 12, ch = u % 12; if (u < 1920) *(LAS u32x4*)(L + L_WQ + row * 192 + (row >> 3) * 32 + ch * 16) = SQ[k]; }
#pragma unroll
    for (int k = 0; k < 4; ++k) { const int u = tid + 512 * k, row = u / 10, ch = u % 10; if (u < 1920) *(LAS u32x4*)(L + L_WO + row * 160 + (row >> 3) * 128 + ch * 16) = SO[k]; }
  };
  const int trq = (c >> 2), trp = (c & 3);

#pragma unroll
  for (int n = 0; n < W_WIN; ++n) LDK(n);
  SCHED();
  prologue_finish();
  wg_barrier();
  STAMP(1);
  const unsigned tag = MISC[0];
#pragma unroll
  for (int n = W_WIN; n < 24; ++n) { pace<W_WIN - 1>(w); LDK(n); SCHED(); }
  if (w == 0) {
    if (isMod) {
      f32x4 a0 = {0, 0, 0, 0}, a1 = {0, 0, 0, 0};
      LAS unsigned char* pa = L + L_A + (c & 3) * 1344 + g * 16;
      LAS unsigned char* pb = L + L_RL + g * 544 + trq * 64 + trp * 8;
#pragma unroll
      for (int ks = 0; ks < 20; ++ks) {
        const u32x4 af = *(LAS u32x4*)(pa + ks * 64);
        a0 = mfma32(af, trfrag(pb + ks * 2176, 256), a0);
        a1 = mfma32(af, trfrag(pb + ks * 2176 + 32, 256), a1);
      }
      const float v0 = rbf(sel4(a0, g)), v1 = rbf(sel4(a1, g));
      if (isHU) {
        const float mb = u2f(MISC[4 + 2 * g]), den = u2f(MISC[5 + 2 * g]);
        const float hv = rbf(rbf(rbf(rbf(((c & 1) ? bfhi(xh) : bflo(xh)) - mb) / den) * rbf(1.f + v1)) + v0);
        const float hn = dpp<XOR1>(hv);
        if ((c & 1) == 0) stsc8(rws, WS_HG + (g * 320 + 8 * hu + (c >> 1)) * 8, u32x2{pack2(hv, hn), tag});
      } else {
        const float n0 = dpp<XOR1>(v0), n1 = dpp<XOR1>(v1);
        const unsigned pv = (c & 1) ? pack2(n1, v1) : pack2(v0, n0);
        stsc8(rws, WS_MG + (g * 1280 + 16 * ru + (c & 1) * 8 + (c >> 1)) * 8, u32x2{pv, tag});
      }
    }
    STAMP(2);
    {
      u32x4 t[3];
      int off[3];
#pragma unroll
      for (int k = 0; k < 3; ++k) { const int n = min(lane + 64 * k, 159); off[k] = WS_HG + (n / 40) * 2560 + b * 640 + (n % 40) * 16; }
#pragma unroll 1
      for (unsigned spins = 0; spins < SPIN_CAP; ++spins) {
        MEMBAR();
#pragma unroll
        for (int k = 0; k < 3; ++k) t[k] = ldsc(rws, off[k]);
        if (all0(tag2(t[0], tag) | tag2(t[1], tag) | tag2(t[2], tag))) break;
        __builtin_amdgcn_s_sleep(4);
      }
#pragma unroll
      for (int k = 0; k < 3; ++k) { const int n = min(lane + 64 * k, 159); *(LAS u32x2*)(L + L_HA + (n / 40) * 320 + (n % 40) * 8) = u32x2{t[k].x, t[k].z}; }
    }
    STAMP(3);
    {
      f32x4 acc[6];
#pragma unroll
      for (int t = 0; t < 6; ++t) acc[t] = f32x4{0, 0, 0, 0};
      LAS unsigned char* pa = L + L_HA + (c & 3) * 320 + g * 16;
      LAS unsigned char* pb = L + L_WQ + g * 1568 + trq * 192 + trp * 8;
#pragma unroll
      for (int ks = 0; ks < 5; ++ks) {
        const u32x4 af = *(LAS u32x4*)(pa + ks * 64);
#pragma unroll
        for (int t = 0; t < 6; ++t) acc[t] = mfma32(af, trfrag(pb + ks * 6272 + t * 32, 768), acc[t]);
      }
#pragma unroll
      for (int tp = 0; tp < 3; ++tp)
        stsc(rws, WS_QG + ((((h * 4 + g) * 4 + b) * 2 + s) * 48 + tp * 16 + c) * 16, u32x4{f2u(sel4(acc[2 * tp], g)), tag, f2u(sel4(acc[2 * tp + 1], g)), tag});
    }
    STAMP(4);
    {
      u32x4 t[6];
      const int qb = WS_QG + (h * 4 + b) * 384 * 16;
      const int o1 = qb + lane * 16, o2 = qb + ((lane >> 5) * 192 + 64 + (lane & 31)) * 16;
#pragma unroll 1
      for (unsigned spins = 0; spins < SPIN_CAP; ++spins) {
        MEMBAR();
#pragma unroll
        for (int k = 0; k < 4; ++k) t[k] = ldsc(rws, o1 + k * 1536);
#pragma unroll
        for (int k = 0; k < 2; ++k) t[4 + k] = ldsc(rws, o2 + k * 1536);
        unsigned bad = 0;
#pragma unroll
        for (int k = 0; k < 6; ++k) bad |= tag2(t[k], tag);
        if (all0(bad)) break;
        __builtin_amdgcn_s_sleep(4);
      }
      const float qa = (u2f(t[0].x) + u2f(t[1].x)) + (u2f(t[2].x) + u2f(t[3].x)), qb2 = (u2f(t[0].z) + u2f(t[1].z)) + (u2f(t[2].z) + u2f(t[3].z));
      float qc = u2f(t[4].x) + u2f(t[5].x), qd = u2f(t[4].z) + u2f(t[5].z);
      { auto r = __builtin_amdgcn_permlane32_swap(f2u(qc), f2u(qc), false, false); qc = u2f(r[0]) + u2f(r[1]); }
      { auto r = __builtin_amdgcn_permlane32_swap(f2u(qd), f2u(qd), false, false); qd = u2f(r[0]) + u2f(r[1]); }
      LAS u16* Q = (LAS u16*)(L + L_Q);
      const int d1 = lane < 48 ? 32 * g + c : 96 + c;
      Q[d1] = (u16)pack2(qa, 0.f); Q[d1 + 16] = (u16)pack2(qb2, 0.f);
      const int d2 = 128 + 32 * (g & 1) + c;
      if (lane < 32) { Q[d2] = (u16)pack2(qc, 0.f); Q[d2 + 16] = (u16)pack2(qd, 0.f); }
      asm volatile("s_waitcnt lgkmcnt(0)" ::: "memory");
      MISC[1] = 1u;
    }
  }
  SCHED();
#pragma unroll
  for (int n = 0; n < NV0; ++n) { pace<W_WIN - 1>(w); LDV(n); SCHED(); }
  STAMP(5);
#pragma unroll 1
  for (unsigned spins = 0; MISC[1] == 0u && spins < (SPIN_CAP << 4); ++spins) __builtin_amdgcn_s_sleep(2);
  STAMP(13);
  SCHED();
#pragma unroll
  for (int n = NV0; n < 24; ++n) LDV(n);
  SCHED();

  unsigned pl;
  {
    LAS unsigned char* pq = L + L_Q;
    const u32x4 q0 = *(LAS u32x4*)(pq + c * 16), q1 = *(LAS u32x4*)(pq + ((16 + c) % 24) * 16), q2 = *(LAS u32x4*)(pq + (8 + c) * 16);
    float v[16];
#pragma unroll
    for (int a = 0; a < 8; ++a) {
      float d1 = 0.f;
#pragma unroll
      for (int k = 0; k < 4; ++k) d1 = dot2(K[3 * a + 1][k], q1[k], d1);
      float e = c < 8 ? d1 : 0.f, o = c < 8 ? 0.f : d1;
#pragma unroll
      for (int k = 0; k < 4; ++k) { e = dot2(K[3 * a][k], q0[k], e); o = dot2(K[3 * a + 2][k], q2[k], o); }
      v[2 * a] = e; v[2 * a + 1] = o;
    }
#define TR_STEP(HALF, CTRL) _Pragma("unroll") for (int k = 0; k < (HALF); ++k) { \
      const float t1 = v[k] + dpp<CTRL>(v[k]), t2 = v[k + (HALF)] + dpp<CTRL>(v[k + (HALF)]); v[k] = (c & (HALF)) ? t2 : t1; }
    TR_STEP(8, XOR8) TR_STEP(4, XOR7) TR_STEP(2, XOR2) TR_STEP(1, XOR1)
#undef TR_STEP
    const float sc = rbf(rbf(v[0]) * 0.07216878364870322f);
    const float mw = xrow_max(row_max(sc));
    const float p = __expf(sc - mw);
    const float lw = wave_sum(p);
    pl = pack2(p, 0.f);
    if (lane == 0) { MISC[16 + w] = f2u(mw); MISC[24 + w] = f2u(lw); }
    asm volatile("s_waitcnt lgkmcnt(0)" ::: "memory");
    unsigned old = 0;
    if (lane == 0) old = __hip_atomic_fetch_add((LAS unsigned*)(L + L_MISC + 8), 1u, __ATOMIC_RELAXED, __HIP_MEMORY_SCOPE_WORKGROUP);
    if (__builtin_amdgcn_readfirstlane(old) == 7u) {
      const float m = u2f(MISC[16 + (c & 7)]), l = u2f(MISC[24 + (c & 7)]);
      const float M = row_max(m);
      const float Ls = row_sum(l * __expf(m - M)) * 0.5f;
      if (lane == 0) stsc(rws, WS_ML + (h * 8 + j8) * 16, u32x4{f2u(M), tag, f2u(Ls), tag});
    }
  }
  STAMP(6);
  SCHED();
  u32x4 BO[6];
  if (w < 5) {
    LAS unsigned char* pb = L + L_WO + g * 1408 + trq * 160 + trp * 8 + w * 32;
#pragma unroll
    for (int ks = 0; ks < 6; ++ks) BO[ks] = trfrag(pb + ks * 5632, 640);
  }
  float acc[24];
#pragma unroll
  for (int k = 0; k < 24; ++k) acc[k] = 0.f;
  const unsigned ph = pl << 16;
  float fsc = 0.f;
#define PV(a) { \
    const unsigned el = dppu<0x150 + 2 * (a)>(pl), ol = dppu<0x151 + 2 * (a)>(pl), eh = dppu<0x150 + 2 * (a)>(ph), oh = dppu<0x151 + 2 * (a)>(ph); \
    const unsigned ml = c < 8 ? el : ol, mh = c < 8 ? eh : oh; \
    _Pragma("unroll") for (int k = 0; k < 4; ++k) { \
      acc[2 * k] = dot2(V[3 * (a)][k], el, acc[2 * k]); acc[2 * k + 1] = dot2(V[3 * (a)][k], eh, acc[2 * k + 1]); \
      acc[8 + 2 * k] = dot2(V[3 * (a) + 1][k], ml, acc[8 + 2 * k]); acc[9 + 2 * k] = dot2(V[3 * (a) + 1][k], mh, acc[9 + 2 * k]); \
      acc[16 + 2 * k] = dot2(V[3 * (a) + 2][k], ol, acc[16 + 2 * k]); acc[17 + 2 * k] = dot2(V[3 * (a) + 2][k], oh, acc[17 + 2 * k]); } \
    SCHED(); }
  PV(0) PV(1) PV(2) PV(3) PV(4) PV(5)
  {
#pragma unroll 1
    for (unsigned spins = 0; MISC[2] < 8u && spins < (SPIN_CAP << 4); ++spins) __builtin_amdgcn_s_sleep(1);
    const float m = u2f(MISC[16 + (c >> 1)]);
    fsc = __expf(m - row_max(m));
  }
  SCHED();
  PV(6) PV(7)
#undef PV
  STAMP(7);
  {
    float u[12], z[6];
#pragma unroll
    for (int k = 0; k < 12; ++k) { auto r = __builtin_amdgcn_permlane32_swap(f2u(acc[k]), f2u(acc[k + 12]), false, false); u[k] = u2f(r[0]) + u2f(r[1]); }
#pragma unroll
    for (int k = 0; k < 6; ++k) { auto r = __builtin_amdgcn_permlane16_swap(f2u(u[k]), f2u(u[k + 6]), false, false); z[k] = u2f(r[0]) + u2f(r[1]); }
    LAS float* op = (LAS float*)(L + L_OP + w * 1552 + g * 384 + c * 4);
#pragma unroll
    for (int k = 0; k < 6; ++k) op[k * 16] = z[k];
  }
  wg_barrier();
  {
    float ov[6];
#pragma unroll
    for (int k = 0; k < 6; ++k) {
      const int d = 24 * w + 6 * g + k, ck = 24 * (c & 1) + (d >> 3);
      ov[k] = *(LAS float*)(L + L_OP + (c >> 1) * 1552 + ((ck >> 4) * 8 + (d & 7)) * 64 + (ck & 15) * 4) * fsc;
    }
#pragma unroll
    for (int k = 0; k < 6; ++k) ov[k] = row_sum(ov[k]);
    const float lo = c == 0 ? ov[0] : c == 1 ? ov[2] : ov[4], hi = c == 0 ? ov[1] : c == 1 ? ov[3] : ov[5];
    if (c < 3) stsc(rws, WS_OG + ((h * 8 + j8) * 96 + 12 * w + 3 * g + c) * 16, u32x4{f2u(lo), tag, f2u(hi), tag});
  }
  STAMP(8);
  if (w < 6) {
    const int ob = tid / 96, ou = tid % 96;
    const int o0 = WS_OG + ((h * 8 + 2 * ob) * 96 + ou) * 16, m0 = WS_ML + (h * 8 + 2 * ob) * 16;
    u32x4 t0, t1, ma, mb;
#pragma unroll 1
    for (unsigned spins = 0; spins < SPIN_CAP; ++spins) {
      MEMBAR();
      t0 = ldsc(rws, o0); t1 = ldsc(rws, o0 + 1536); ma = ldsc(rws, m0); mb = ldsc(rws, m0 + 16);
      if (all0(tag2(t0, tag) | tag2(t1, tag) | tag2(ma, tag) | tag2(mb, tag))) break;
      __builtin_amdgcn_s_sleep(1);
    }
    const float M = fmaxf(u2f(ma.x), u2f(mb.x)), e0 = __expf(u2f(ma.x) - M), e1 = __expf(u2f(mb.x) - M);
    const float inv = 1.f / (u2f(ma.z) * e0 + u2f(mb.z) * e1), w0 = e0 * inv, w1 = e1 * inv;
    *(LAS unsigned*)(L + L_OA + ob * 448 + ou * 4) = pack2(u2f(t0.x) * w0 + u2f(t1.x) * w1, u2f(t0.z) * w0 + u2f(t1.z) * w1);
  }
  wg_barrier();
  STAMP(9);
  if (w < 5) {
    f32x4 y = {0, 0, 0, 0};
    LAS unsigned char* pa = L + L_OA + (c & 3) * 448 + g * 16;
#pragma unroll
    for (int ks = 0; ks < 6; ++ks) y = mfma32(*(LAS u32x4*)(pa + ks * 64), BO[ks], y);
    stsc8(rws, WS_YG + ((g * 40 + 5 * j8 + w) * 32 + h) * 128 + c * 8, u32x2{f2u(sel4(y, g)), tag});
  }
  STAMP(10);
  if (isRed) {
    float x1a = 0.f, x1b = 0.f; unsigned gb2 = 0;
    if (w == 0) {
      u32x4 t[4]; u32x2 ga, gb;
      const int yo = WS_YG + (rb * 40 + rt) * 4096 + lane * 16, mo = WS_MG + (rb * 1280 + 8 * rt + (lane & 7)) * 8;
#pragma unroll 1
      for (unsigned spins = 0; spins < SPIN_CAP; ++spins) {
        MEMBAR();
#pragma unroll
        for (int k = 0; k < 4; ++k) t[k] = ldsc(rws, yo + k * 1024);
        ga = ldsc8(rws, mo); gb = ldsc8(rws, mo + 960 * 8);
        if (all0(tag2(t[0], tag) | tag2(t[1], tag) | tag2(t[2], tag) | tag2(t[3], tag) | (ga.y ^ tag) | (gb.y ^ tag))) break;
        __builtin_amdgcn_s_sleep(1);
      }
      float ya = (u2f(t[0].x) + u2f(t[1].x)) + (u2f(t[2].x) + u2f(t[3].x)), yb = (u2f(t[0].z) + u2f(t[1].z)) + (u2f(t[2].z) + u2f(t[3].z));
      ya += dpp<XOR8>(ya); yb += dpp<XOR8>(yb);
      ya = xrow_sum(ya); yb = xrow_sum(yb);
      x1a = rbf(bflo(xres) + rbf(bflo(ga.x) * rbf(ya))); x1b = rbf(bfhi(xres) + rbf(bfhi(ga.x) * rbf(yb)));
      gb2 = gb.x;
      if (lane < 8) stsc8(rws, WS_XG + (rb * 320 + 8 * rt + lane) * 8, u32x2{pack2(x1a, x1b), tag});
    }
    STAMP(11);
    if (w < 6) {
      u32x4 t0, t1;
      const int po = WS_PG + (rb * 40 + rt) * 12288 + tid * 16;
#pragma unroll 1
      for (unsigned spins = 0; spins < SPIN_CAP; ++spins) {
        MEMBAR();
        t0 = ldsc(rws, po); t1 = ldsc(rws, po + 6144);
        if (all0(tag2(t0, tag) | tag2(t1, tag))) break;
        __builtin_amdgcn_s_sleep(2);
      }
      float ya = u2f(t0.x) + u2f(t1.x), yb = u2f(t0.z) + u2f(t1.z);
      ya += dpp<XOR8>(ya); yb += dpp<XOR8>(yb);
      ya = xrow_sum(ya); yb = xrow_sum(yb);
      if (lane < 8) *(LAS u32x2*)(L + L_RED + w * 64 + lane * 8) = u32x2{f2u(ya), f2u(yb)};
    }
    wg_barrier();
    if (tid < 8) {
      float ya = 0.f, yb = 0.f;
#pragma unroll
      for (int k = 0; k < 6; ++k) { const u32x2 r = *(LAS u32x2*)(L + L_RED + k * 64 + lane * 8); ya += u2f(r.x); yb += u2f(r.y); }
      ((unsigned*)out)[rb * 320 + 8 * rt + lane] = pack2(x1a + rbf(bflo(gb2) * rbf(ya)), x1b + rbf(bfhi(gb2) * rbf(yb)));
      if (bid == 0 && tid == 0) __hip_atomic_store((GAS unsigned*)(ws + WS_EPOCH), epoch + 1u, __ATOMIC_RELAXED, __HIP_MEMORY_SCOPE_AGENT);
    }
    if (bid == 0 && w == 0) {
      unsigned long long mn = ~0ull, mx = 0;
      for (int k = 0; k < 4; ++k) { const unsigned long long t = __hip_atomic_load((GAS unsigned long long*)(ws + WS_STAMP) + lane + 64 * k, __ATOMIC_RELAXED, __HIP_MEMORY_SCOPE_AGENT); mn = t < mn ? t : mn; mx = t > mx ? t : mx; }
      unsigned lo = (unsigned)(pt0 - mn), hi = (unsigned)(mx - pt0 + 100000);
      for (int o = 32; o; o >>= 1) { lo = max(lo, (unsigned)__shfl_xor((int)lo, o)); hi = max(hi, (unsigned)__shfl_xor((int)hi, o)); }
      hi -= 100000;
      const unsigned long long now = __builtin_amdgcn_s_memrealtime();
      const unsigned d = PROBE == 1 ? lo : PROBE == 2 ? hi : PROBE == 3 ? (unsigned)(now - pt0) : 0u;
      const unsigned long long te = now + (PROBE == 3 ? 1 : 10) * (unsigned long long)__builtin_amdgcn_readfirstlane(d);
      while (__builtin_amdgcn_s_memrealtime() < te) __builtin_amdgcn_s_sleep(1);
    }
  } else {
    u32x4 BG[5]; u32x2 BD[5];
    {
      LAS unsigned char* pb = L + L_RL + (w >> 1) * (5 * 2176) + g * 544 + trq * 64 + trp * 8 + (w & 1) * 32;
#pragma unroll
      for (int ks = 0; ks < 5; ++ks) BG[ks] = trfrag(pb + ks * 2176, 256);
      LAS unsigned char* pd = L + L_WD + (4 * g + trq) * 1312 + trp * 8 + w * 160;
#pragma unroll
      for (int t = 0; t < 5; ++t) BD[t] = trread(pd + t * 32);
    }
    if (w < 4) {
      u32x4 sb[3], cb[3], t[3];
      int oo[3];
#pragma unroll
      for (int k = 0; k < 3; ++k) oo[k] = min(lane + 64 * k, 159) * 16;
#pragma unroll 1
      for (unsigned spins = 0; spins < SPIN_CAP; ++spins) {
        MEMBAR();
        unsigned bad = 0;
#pragma unroll
        for (int k = 0; k < 3; ++k) { sb[k] = ldsc(rws, WS_MG + w * 10240 + 2560 + oo[k]); cb[k] = ldsc(rws, WS_MG + w * 10240 + 5120 + oo[k]); }
#pragma unroll
        for (int k = 0; k < 3; ++k) bad |= tag2(sb[k], tag) | tag2(cb[k], tag);
        if (all0(bad)) break;
        __builtin_amdgcn_s_sleep(2);
      }
#pragma unroll 1
      for (unsigned spins = 0; spins < SPIN_CAP; ++spins) {
        MEMBAR();
#pragma unroll
        for (int k = 0; k < 3; ++k) t[k] = ldsc(rws, WS_XG + w * 2560 + oo[k]);
        if (all0(tag2(t[0], tag) | tag2(t[1], tag) | tag2(t[2], tag))) break;
        __builtin_amdgcn_s_sleep(1);
      }
      STAMP(11);
      float xv[12];
#pragma unroll
      for (int k = 0; k < 3; ++k) { xv[4 * k] = bflo(t[k].x); xv[4 * k + 1] = bfhi(t[k].x); xv[4 * k + 2] = bflo(t[k].z); xv[4 * k + 3] = bfhi(t[k].z); }
      const bool l3 = lane < 32;
      float sm = ((xv[0] + xv[1]) + (xv[2] + xv[3])) + ((xv[4] + xv[5]) + (xv[6] + xv[7]));
      sm += l3 ? (xv[8] + xv[9]) + (xv[10] + xv[11]) : 0.f;
      const float mean = wave_sum(sm) * (1.f / D);
      float sv = 0.f, sv3 = 0.f;
#pragma unroll
      for (int k = 0; k < 8; ++k) { const float dlt = xv[k] - mean; sv = fmaf(dlt, dlt, sv); }
#pragma unroll
      for (int k = 8; k < 12; ++k) { const float dlt = xv[k] - mean; sv3 = fmaf(dlt, dlt, sv3); }
      sv += l3 ? sv3 : 0.f;
      const float var = wave_sum(sv) * (1.f / D);
      const float mb = rbf(mean), den = rbf(sqrtf(rbf(rbf(var) + 1e-5f)));
#pragma unroll
      for (int k = 0; k < 3; ++k) {
        const float s0 = bflo(sb[k].x), s1 = bfhi(sb[k].x), s2 = bflo(sb[k].z), s3 = bfhi(sb[k].z);
        const float c0 = bflo(cb[k].x), c1 = bfhi(cb[k].x), c2 = bflo(cb[k].z), c3 = bfhi(cb[k].z);
        const float h0 = rbf(rbf(rbf(xv[4 * k] - mb) / den) * rbf(1.f + c0)) + s0, h1 = rbf(rbf(rbf(xv[4 * k + 1] - mb) / den) * rbf(1.f + c1)) + s1;
        const float h2 = rbf(rbf(rbf(xv[4 * k + 2] - mb) / den) * rbf(1.f + c2)) + s2, h3 = rbf(rbf(rbf(xv[4 * k + 3] - mb) / den) * rbf(1.f + c3)) + s3;
        *(LAS u32x2*)(L + L_A + w * 1344 + (oo[k] >> 1)) = u32x2{pack2(h0, h1), pack2(h2, h3)};
      }
    }
    wg_barrier();
    {
      f32x4 a = {0, 0, 0, 0};
      LAS unsigned char* pa = L + L_A + (c & 3) * 1344 + (w >> 1) * 320 + g * 16;
#pragma unroll
      for (int ks = 0; ks < 5; ++ks) a = mfma32(*(LAS u32x4*)(pa + ks * 64), BG[ks], a);
      *(LAS float*)(L + L_RED + w * 256 + lane * 4) = sel4(a, g);
    }
    wg_barrier();
    {
      f32x4 gs = {0, 0, 0, 0}, us = {0, 0, 0, 0};
#pragma unroll
      for (int k = 0; k < 4; ++k) {
        gs += *(LAS f32x4*)(L + L_RED + (2 * k) * 256 + (c & 3) * 64 + g * 16);
        us += *(LAS f32x4*)(L + L_RED + (2 * k + 1) * 256 + (c & 3) * 64 + g * 16);
      }
      float hh[4];
#pragma unroll
      for (int k = 0; k < 4; ++k) { const float gv = rbf(gs[k]); hh[k] = gv * (gv > 0.f ? 1.f : 0.f) * rbf(us[k]); }
      const u32x2 af = {pack2(hh[0], hh[1]), pack2(hh[2], hh[3])};
#pragma unroll
      for (int t = 0; t < 5; ++t) {
        f32x4 y = {0, 0, 0, 0};
        y = __builtin_amdgcn_mfma_f32_16x16x16bf16_1k(__builtin_bit_cast(s16x4, af), __builtin_bit_cast(s16x4, BD[t]), y, 0, 0, 0);
        stsc8(rws, WS_PG + ((g * 40 + 5 * w + t) * 96 + mid) * 128 + c * 8, u32x2{f2u(sel4(y, g)), tag});
      }
    }
    STAMP(12);
  }
}

extern "C" void kernel_launch(void* const* d_in, const int* in_sizes, int n_in, void* d_out, int out_size, void* d_ws, size_t ws_size, hipStream_t stream) {
  fused_block<<<256, 512, 0, stream>>>((const u16*)d_in[0], (const u16*)d_in[1], (const u16*)d_in[2], (const u16*)d_in[3], (const u16*)d_in[4],
                                       (const u16*)d_in[6], (const u16*)d_in[7], (const u16*)d_in[8], (const u16*)d_in[9], (const u16*)d_in[10],
                                       (u16*)d_out, (unsigned char*)d_ws);
}
```

```cpp
#define PROBE 3
#include <hip/hip_runtime.h>
#include <hip/hip_bf16.h>
#include <stdint.h>

#define W_WIN 5
#ifndef NV0
#define NV0 9
#endif
#define AUX_K 2
#define AUX_V 2
#define AUX_W 2
#define STAMPS 0

typedef unsigned short u16;
typedef unsigned u32x4 __attribute__((ext_vector_type(4)));
typedef unsigned u32x2 __attribute__((ext_vector_type(2)));
typedef float f32x4 __attribute__((ext_vector_type(4)));
typedef short s16x4 __attribute__((ext_vector_type(4)));
typedef short s16x8 __attribute__((ext_vector_type(8)));
typedef __bf16 bf16x2 __attribute__((ext_vector_type(2)));
typedef __bf16 bf16x8 __attribute__((ext_vector_type(8)));
#define LAS __attribute__((address_space(3)))
#define GAS __attribute__((address_space(1)))
typedef __amdgpu_buffer_rsrc_t rsrc_t;

constexpr int D = 640, NH = 32, DH = 192, SK = 4096, WIN = 1024, FF = 1536;
constexpr int AUX_NT = 2, AUX_SC1 = 16;
constexpr unsigned SPIN_CAP = 1u << 16;

constexpr int WS_EPOCH = 0;
constexpr int WS_HG = 1024;
constexpr int WS_XG = WS_HG + 4 * 320 * 8;
constexpr int WS_MG = WS_XG + 4 * 320 * 8;
constexpr int WS_ML = WS_MG + 4 * 1280 * 8;
constexpr int WS_OG = WS_ML + 256 * 16;
constexpr int WS_QG = WS_OG + 256 * 96 * 16;
constexpr int WS_YG = WS_QG + 32 * 4 * 384 * 16;
constexpr int WS_PG = WS_YG + 4 * 40 * 32 * 128;
constexpr int WS_END = WS_PG + 4 * 40 * 96 * 128;
constexpr int WS_STAMP = 8 << 20;

constexpr int L_WQ = 0;
constexpr int L_WO = L_WQ + 160 * 192 + 20 * 32;
constexpr int L_RL = L_WO + 192 * 160 + 24 * 128;
constexpr int L_WD = L_RL + 80 * 544;
constexpr int L_A = L_WD + 16 * 1312;
constexpr int L_HA = L_A + 4 * 1344;
constexpr int L_OA = L_HA + 4 * 320;
constexpr int L_Q = L_OA + 4 * 448;
constexpr int L_OP = L_Q + 384;
constexpr int L_RED = L_OP + 8 * 1552;
constexpr int L_MISC = L_RED + 8 * 256;
constexpr int L_BYTES = L_MISC + 256;
static_assert(L_BYTES <= 160 * 1024 && L_BYTES > 80 * 1024, "one workgroup per CU");

__device__ __forceinline__ float bflo(unsigned u) { return __builtin_bit_cast(float, u << 16); }
__device__ __forceinline__ float bfhi(unsigned u) { return __builtin_bit_cast(float, u & 0xffff0000u); }
__device__ __forceinline__ float rbf(float f) { return (float)(__bf16)f; }
__device__ __forceinline__ unsigned pack2(float lo, float hi) { bf16x2 v = {(__bf16)lo, (__bf16)hi}; return __builtin_bit_cast(unsigned, v); }
__device__ __forceinline__ unsigned f2u(float f) { return __builtin_bit_cast(unsigned, f); }
__device__ __forceinline__ float u2f(unsigned u) { return __builtin_bit_cast(float, u); }
__device__ __forceinline__ float dot2(unsigned a, unsigned b, float c) {
  return __builtin_amdgcn_fdot2_f32_bf16(__builtin_bit_cast(bf16x2, a), __builtin_bit_cast(bf16x2, b), c, false);
}
template <int CTRL> __device__ __forceinline__ float dpp(float x) { return u2f((unsigned)__builtin_amdgcn_mov_dpp((int)f2u(x), CTRL, 0xf, 0xf, true)); }
template <int CTRL> __device__ __forceinline__ unsigned dppu(unsigned x) { return (unsigned)__builtin_amdgcn_mov_dpp((int)x, CTRL, 0xf, 0xf, true); }
constexpr int XOR1 = 0xB1, XOR2 = 0x4E, XOR7 = 0x141, XOR8 = 0x128, XOR15 = 0x140;
__device__ __forceinline__ float row_sum(float v) { v += dpp<XOR1>(v); v += dpp<XOR2>(v); v += dpp<XOR7>(v); v += dpp<XOR8>(v); return v; }
__device__ __forceinline__ float row_max(float v) { v = fmaxf(v, dpp<XOR1>(v)); v = fmaxf(v, dpp<XOR2>(v)); v = fmaxf(v, dpp<XOR7>(v)); v = fmaxf(v, dpp<XOR8>(v)); return v; }
__device__ __forceinline__ float xrow_sum(float x) {
  auto s = __builtin_amdgcn_permlane16_swap(f2u(x), f2u(x), false, false);
  x = u2f(s[0]) + u2f(s[1]);
  auto t = __builtin_amdgcn_permlane32_swap(f2u(x), f2u(x), false, false);
  return u2f(t[0]) + u2f(t[1]);
}
__device__ __forceinline__ float xrow_max(float x) {
  auto s = __builtin_amdgcn_permlane16_swap(f2u(x), f2u(x), false, false);
  x = fmaxf(u2f(s[0]), u2f(s[1]));
  auto t = __builtin_amdgcn_permlane32_swap(f2u(x), f2u(x), false, false);
  return fmaxf(u2f(t[0]), u2f(t[1]));
}
__device__ __forceinline__ float wave_sum(float v) { return xrow_sum(row_sum(v)); }
__device__ __forceinline__ rsrc_t mkrs(const void* p, unsigned bytes) { return __builtin_amdgcn_make_buffer_rsrc((void*)p, 0, bytes, 0x00020000); }
template <int AUX> __device__ __forceinline__ u32x4 ldx(rsrc_t rs, int voff, int soff) { return __builtin_amdgcn_raw_buffer_load_b128(rs, voff, soff, AUX); }
#define ldnt ldx<AUX_W>
__device__ __forceinline__ u32x4 ldsc(rsrc_t rs, int voff) { return __builtin_amdgcn_raw_buffer_load_b128(rs, voff, 0, AUX_SC1); }
__device__ __forceinline__ u32x2 ldsc8(rsrc_t rs, int voff) { return __builtin_amdgcn_raw_buffer_load_b64(rs, voff, 0, AUX_SC1); }
__device__ __forceinline__ void stsc(rsrc_t rs, int voff, u32x4 v) { __builtin_amdgcn_raw_buffer_store_b128(v, rs, voff, 0, AUX_SC1); }
__device__ __forceinline__ void stsc8(rsrc_t rs, int voff, u32x2 v) { __builtin_amdgcn_raw_buffer_store_b64(v, rs, voff, 0, AUX_SC1); }
template <int N> __device__ __forceinline__ void waitvm() { asm volatile("s_waitcnt vmcnt(%0)" ::"i"(N) : "memory"); }
template <int N> __device__ __forceinline__ void pace(int w) {
  asm volatile("s_cmp_eq_u32 %0, 0\n\ts_cbranch_scc1 1f\n\ts_waitcnt vmcnt(%1)\n1:" ::"s"(w), "i"(N) : "scc", "memory");
}
#define SCHED() __builtin_amdgcn_sched_barrier(0)
#define MEMBAR() asm volatile("" ::: "memory")
__device__ __forceinline__ void wg_barrier() { asm volatile("s_waitcnt lgkmcnt(0)\n\ts_barrier" ::: "memory"); }
__device__ __forceinline__ f32x4 mfma32(u32x4 a, u32x4 b, f32x4 c) {
  return __builtin_amdgcn_mfma_f32_16x16x32_bf16(__builtin_bit_cast(bf16x8, a), __builtin_bit_cast(bf16x8, b), c, 0, 0, 0);
}
__device__ __forceinline__ u32x2 trread(LAS unsigned char* p) { return __builtin_bit_cast(u32x2, __builtin_amdgcn_ds_read_tr16_b64_v4i16((LAS s16x4*)p)); }
__device__ __forceinline__ u32x4 trfrag(LAS unsigned char* p, int step) {
  const u32x2 lo = trread(p), hi = trread(p + step);
  return u32x4{lo.x, lo.y, hi.x, hi.y};
}
__device__ __forceinline__ float sel4(f32x4 v, int g) { const float a = (g & 1) ? v[1] : v[0], b = (g & 1) ? v[3] : v[2]; return (g & 2) ? b : a; }
__device__ __forceinline__ unsigned tag2(u32x4 v, unsigned tag) { return (v.y ^ tag) | (v.w ^ tag); }
__device__ __forceinline__ bool all0(unsigned bad) { return __builtin_amdgcn_ballot_w64(bad != 0u) == 0ull; }

#define STAMP(n) do {} while (0)

__global__ void __launch_bounds__(512, 2)
fused_block(const u16* __restrict__ xin, const u16* __restrict__ kc, const u16* __restrict__ vc, const u16* __restrict__ wq,
            const u16* __restrict__ wo, const u16* __restrict__ wg, const u16* __restrict__ wu, const u16* __restrict__ wd,
            const u16* __restrict__ cin, const u16* __restrict__ wmod, u16* __restrict__ out, unsigned char* ws) {
  __shared__ __attribute__((aligned(16))) unsigned char smem[L_BYTES];
  LAS unsigned char* L = (LAS unsigned char*)smem;
  volatile LAS unsigned* MISC = (volatile LAS unsigned*)(L + L_MISC);
  const int tid = threadIdx.x, lane = tid & 63, w = __builtin_amdgcn_readfirstlane(tid >> 6), g = lane >> 4, c = lane & 15;
  const int bid = blockIdx.x, x = bid & 7, slot = bid >> 3, h = 4 * x + (slot >> 3), j8 = slot & 7, b = j8 >> 1, s = j8 & 1;
  const bool isMod = slot < 15, isHU = isMod && x < 4 && slot < 10, isRed = slot < 20, isMlp = slot >= 20;
  const int hu = 10 * x + slot;
  const int ru = x < 4 ? 5 * x + (slot - 10) : 20 + 15 * (x - 4) + slot;
  const int rid = x * 20 + slot, rb = rid / 40, rt = rid % 40;
  const int mid = x * 12 + (slot - 20);
  unsigned wbd = 0;
  if ((PROBE == 1 || PROBE == 2) && bid < 8 && w == 7) {
    const unsigned long long t0 = __builtin_amdgcn_s_memrealtime();
    asm volatile("buffer_wbl2 sc1\n\ts_waitcnt vmcnt(0)" ::: "memory");
    wbd = (unsigned)(__builtin_amdgcn_s_memrealtime() - t0);
  }

  const rsrc_t rws = mkrs(ws, WS_END);
  const size_t kvbase = ((size_t)(b * NH + h) * SK + (SK - WIN) + s * 512 + w * 64) * DH;
  const rsrc_t rk = mkrs(kc + kvbase, 64 * 384), rv = mkrs(vc + kvbase, 64 * 384);
  const int kvo = g * 768 + c * 16;
  u32x4 K[24], V[24];
#define LDK(n) K[n] = ldx<AUX_K>(rk, kvo + ((n) % 3) * 256, ((n) / 3) * 3072)
#define LDV(n) V[n] = ldx<AUX_V>(rv, kvo + ((n) % 3) * 256, ((n) / 3) * 3072)

  u32x4 SR[8], SQ[4], SO[4];
  if (isMod) {
    const rsrc_t rm = mkrs(wmod, D * 6 * D * 2);
    const int c0 = isHU ? 16 * hu : 1280 + 32 * ru, c1 = isHU ? 640 + 16 * hu : 1296 + 32 * ru;
    const int ch = tid & 3, vo = (tid >> 2) * 7680 + ((ch & 2) ? c1 : c0) * 2 + (ch & 1) * 16;
#pragma unroll
    for (int k = 0; k < 5; ++k) SR[k] = ldnt(rm, vo, k * 128 * 7680);
  } else if (isMlp) {
    const rsrc_t rg = mkrs(w < 4 ? wg : wu, D * FF * 2), rd = mkrs(wd + (size_t)mid * 16 * D, 16 * D * 2);
    const int t2 = tid & 255, vo = (t2 >> 1) * 3072 + mid * 32 + (t2 & 1) * 16;
#pragma unroll
    for (int k = 0; k < 5; ++k) SR[k] = ldnt(rg, vo, k * 128 * 3072);
#pragma unroll
    for (int k = 0; k < 3; ++k) SR[5 + k] = ldnt(rd, tid * 16 + k * 8192, 0);
  }
  {
    const rsrc_t rq = mkrs(wq + (size_t)(160 * b) * (NH * DH) + DH * h + 96 * s, 160 * NH * DH * 2 - (DH * h + 96 * s) * 2);
    const rsrc_t ro = mkrs(wo + (size_t)(DH * h) * D + 80 * j8, DH * D * 2 - 80 * j8 * 2);
#pragma unroll
    for (int k = 0; k < 4; ++k) { const int u = tid + 512 * k, row = u / 12, ch = u % 12; SQ[k] = ldnt(rq, row * 12288 + ch * 16, 0); }
#pragma unroll
    for (int k = 0; k < 4; ++k) { const int u = tid + 512 * k, row = u / 10, ch = u % 10; SO[k] = ldnt(ro, row * 1280 + ch * 16, 0); }
  }
  SCHED();
  unsigned epoch = 0;
  if (w == 0) epoch = __hip_atomic_load((GAS unsigned*)(ws + WS_EPOCH), __ATOMIC_RELAXED, __HIP_MEMORY_SCOPE_AGENT);
  u32x4 cv = {0, 0, 0, 0};
  unsigned xr[5] = {0, 0, 0, 0, 0};
  unsigned xh = 0;
  if (isMod && tid < 320) cv = *(const u32x4*)(cin + tid * 8);
  if (isHU) {
    if (w >= 4) {
#pragma unroll
      for (int k = 0; k < 5; ++k) xr[k] = ((const unsigned*)xin)[(w - 4) * 320 + lane + 64 * k];
    }
    if (w == 0) xh = ((const unsigned*)xin)[g * 320 + 8 * hu + (c >> 1)];
  }
  unsigned xres = 0;
  if (isRed && tid < 8) xres = ((const unsigned*)xin)[rb * 320 + rt * 8 + tid];
  SCHED();
  auto prologue_finish = [&]() {
    if (tid == 0) { MISC[0] = (epoch + 1u) * 0x9E3779B1u; MISC[1] = 0u; MISC[2] = 0u; }
    if (isMod && tid < 320) {
      u32x4 o;
#pragma unroll
      for (int k = 0; k < 4; ++k) { const float a = bflo(cv[k]), bb = bfhi(cv[k]); o[k] = pack2(a / (1.f + __expf(-a)), bb / (1.f + __expf(-bb))); }
      *(LAS u32x4*)(L + L_A + (tid / 80) * 1344 + (tid % 80) * 16) = o;
    }
    if (isHU && w >= 4) {
      float sm = 0.f;
#pragma unroll
      for (int k = 0; k < 5; ++k) sm += bflo(xr[k]) + bfhi(xr[k]);
      const float mean = wave_sum(sm) * (1.f / D);
      float sv = 0.f;
#pragma unroll
      for (int k = 0; k < 5; ++k) { const float a = bflo(xr[k]) - mean, bb = bfhi(xr[k]) - mean; sv += a * a + bb * bb; }
      const float var = wave_sum(sv) * (1.f / D);
      if (lane == 0) { MISC[4 + 2 * (w - 4)] = f2u(rbf(mean)); MISC[5 + 2 * (w - 4)] = f2u(rbf(sqrtf(rbf(rbf(var) + 1e-5f)))); }
    }
    if (isMod) {
#pragma unroll
      for (int k = 0; k < 5; ++k) { const int row = (tid >> 2) + 128 * k; *(LAS u32x4*)(L + L_RL + row * 64 + (row >> 3) * 32 + (tid & 3) * 16) = SR[k]; }
    } else if (isMlp) {
      const int t2 = tid & 255;
#pragma unroll
      for (int k = 0; k < 5; ++k) { const int row = (t2 >> 1) + 128 * k; *(LAS u32x4*)(L + L_RL + row * 64 + (row >> 3) * 32 + (w >> 2) * 32 + (t2 & 1) * 16) = SR[k]; }
#pragma unroll
      for (int k = 0; k < 3; ++k) { const int u = tid + 512 * k; if (u < 1280) *(LAS u32x4*)(L + L_WD + (u / 80) * 1312 + (u % 80) * 16) = SR[5 + k]; }
    }
#pragma unroll
    for (int k = 0; k < 4; ++k) { const int u = tid + 512 * k, row = u / 12, ch = u % 12; if (u < 1920) *(LAS u32x4*)(L + L_WQ + row * 192 + (row >> 3) * 32 + ch * 16) = SQ[k]; }
#pragma unroll
    for (int k = 0; k < 4; ++k) { const int u = tid + 512 * k, row = u / 10, ch = u % 10; if (u < 1920) *(LAS u32x4*)(L + L_WO + row * 160 + (row >> 3) * 128 + ch * 16) = SO[k]; }
  };
  const int trq = (c >> 2), trp = (c & 3);

#pragma unroll
  for (int n = 0; n < W_WIN; ++n) LDK(n);
  SCHED();
  prologue_finish();
  wg_barrier();
  STAMP(1);
  const unsigned tag = MISC[0];
#pragma unroll
  for (int n = W_WIN; n < 24; ++n) { pace<W_WIN - 1>(w); LDK(n); SCHED(); }
  if (w == 0) {
    if (isMod) {
      f32x4 a0 = {0, 0, 0, 0}, a1 = {0, 0, 0, 0};
      LAS unsigned char* pa = L + L_A + (c & 3) * 1344 + g * 16;
      LAS unsigned char* pb = L + L_RL + g * 544 + trq * 64 + trp * 8;
#pragma unroll
      for (int ks = 0; ks < 20; ++ks) {
        const u32x4 af = *(LAS u32x4*)(pa + ks * 64);
        a0 = mfma32(af, trfrag(pb + ks * 2176, 256), a0);
        a1 = mfma32(af, trfrag(pb + ks * 2176 + 32, 256), a1);
      }
      const float v0 = rbf(sel4(a0, g)), v1 = rbf(sel4(a1, g));
      if (isHU) {
        const float mb = u2f(MISC[4 + 2 * g]), den = u2f(MISC[5 + 2 * g]);
        const float hv = rbf(rbf(rbf(rbf(((c & 1) ? bfhi(xh) : bflo(xh)) - mb) / den) * rbf(1.f + v1)) + v0);
        const float hn = dpp<XOR1>(hv);
        if ((c & 1) == 0) stsc8(rws, WS_HG + (g * 320 + 8 * hu + (c >> 1)) * 8, u32x2{pack2(hv, hn), tag});
      } else {
        const float n0 = dpp<XOR1>(v0), n1 = dpp<XOR1>(v1);
        const unsigned pv = (c & 1) ? pack2(n1, v1) : pack2(v0, n0);
        stsc8(rws, WS_MG + (g * 1280 + 16 * ru + (c & 1) * 8 + (c >> 1)) * 8, u32x2{pv, tag});
      }
    }
    STAMP(2);
    {
      u32x4 t[3];
      int off[3];
#pragma unroll
      for (int k = 0; k < 3; ++k) { const int n = min(lane + 64 * k, 159); off[k] = WS_HG + (n / 40) * 2560 + b * 640 + (n % 40) * 16; }
#pragma unroll 1
      for (unsigned spins = 0; spins < SPIN_CAP; ++spins) {
        MEMBAR();
#pragma unroll
        for (int k = 0; k < 3; ++k) t[k] = ldsc(rws, off[k]);
        if (all0(tag2(t[0], tag) | tag2(t[1], tag) | tag2(t[2], tag))) break;
        __builtin_amdgcn_s_sleep(4);
      }
#pragma unroll
      for (int k = 0; k < 3; ++k) { const int n = min(lane + 64 * k, 159); *(LAS u32x2*)(L + L_HA + (n / 40) * 320 + (n % 40) * 8) = u32x2{t[k].x, t[k].z}; }
    }
    STAMP(3);
    {
      f32x4 acc[6];
#pragma unroll
      for (int t = 0; t < 6; ++t) acc[t] = f32x4{0, 0, 0, 0};
      LAS unsigned char* pa = L + L_HA + (c & 3) * 320 + g * 16;
      LAS unsigned char* pb = L + L_WQ + g * 1568 + trq * 192 + trp * 8;
#pragma unroll
      for (int ks = 0; ks < 5; ++ks) {
        const u32x4 af = *(LAS u32x4*)(pa + ks * 64);
#pragma unroll
        for (int t = 0; t < 6; ++t) acc[t] = mfma32(af, trfrag(pb + ks * 6272 + t * 32, 768), acc[t]);
      }
#pragma unroll
      for (int tp = 0; tp < 3; ++tp)
        stsc(rws, WS_QG + ((((h * 4 + g) * 4 + b) * 2 + s) * 48 + tp * 16 + c) * 16, u32x4{f2u(sel4(acc[2 * tp], g)), tag, f2u(sel4(acc[2 * tp + 1], g)), tag});
    }
    STAMP(4);
    {
      u32x4 t[6];
      const int qb = WS_QG + (h * 4 + b) * 384 * 16;
      const int o1 = qb + lane * 16, o2 = qb + ((lane >> 5) * 192 + 64 + (lane & 31)) * 16;
#pragma unroll 1
      for (unsigned spins = 0; spins < SPIN_CAP; ++spins) {
        MEMBAR();
#pragma unroll
        for (int k = 0; k < 4; ++k) t[k] = ldsc(rws, o1 + k * 1536);
#pragma unroll
        for (int k = 0; k < 2; ++k) t[4 + k] = ldsc(rws, o2 + k * 1536);
        unsigned bad = 0;
#pragma unroll
        for (int k = 0; k < 6; ++k) bad |= tag2(t[k], tag);
        if (all0(bad)) break;
        __builtin_amdgcn_s_sleep(4);
      }
      const float qa = (u2f(t[0].x) + u2f(t[1].x)) + (u2f(t[2].x) + u2f(t[3].x)), qb2 = (u2f(t[0].z) + u2f(t[1].z)) + (u2f(t[2].z) + u2f(t[3].z));
      float qc = u2f(t[4].x) + u2f(t[5].x), qd = u2f(t[4].z) + u2f(t[5].z);
      { auto r = __builtin_amdgcn_permlane32_swap(f2u(qc), f2u(qc), false, false); qc = u2f(r[0]) + u2f(r[1]); }
      { auto r = __builtin_amdgcn_permlane32_swap(f2u(qd), f2u(qd), false, false); qd = u2f(r[0]) + u2f(r[1]); }
      LAS u16* Q = (LAS u16*)(L + L_Q);
      const int d1 = lane < 48 ? 32 * g + c : 96 + c;
      Q[d1] = (u16)pack2(qa, 0.f); Q[d1 + 16] = (u16)pack2(qb2, 0.f);
      const int d2 = 128 + 32 * (g & 1) + c;
      if (lane < 32) { Q[d2] = (u16)pack2(qc, 0.f); Q[d2 + 16] = (u16)pack2(qd, 0.f); }
      asm volatile("s_waitcnt lgkmcnt(0)" ::: "memory");
      MISC[1] = 1u;
    }
  }
  SCHED();
#pragma unroll
  for (int n = 0; n < NV0; ++n) { pace<W_WIN - 1>(w); LDV(n); SCHED(); }
  STAMP(5);
#pragma unroll 1
  for (unsigned spins = 0; MISC[1] == 0u && spins < (SPIN_CAP << 4); ++spins) __builtin_amdgcn_s_sleep(2);
  STAMP(13);
  SCHED();
#pragma unroll
  for (int n = NV0; n < 24; ++n) LDV(n);
  SCHED();

  unsigned pl;
  {
    LAS unsigned char* pq = L + L_Q;
    const u32x4 q0 = *(LAS u32x4*)(pq + c * 16), q1 = *(LAS u32x4*)(pq + ((16 + c) % 24) * 16), q2 = *(LAS u32x4*)(pq + (8 + c) * 16);
    float v[16];
#pragma unroll
    for (int a = 0; a < 8; ++a) {
      float d1 = 0.f;
#pragma unroll
      for (int k = 0; k < 4; ++k) d1 = dot2(K[3 * a + 1][k], q1[k], d1);
      float e = c < 8 ? d1 : 0.f, o = c < 8 ? 0.f : d1;
#pragma unroll
      for (int k = 0; k < 4; ++k) { e = dot2(K[3 * a][k], q0[k], e); o = dot2(K[3 * a + 2][k], q2[k], o); }
      v[2 * a] = e; v[2 * a + 1] = o;
    }
#define TR_STEP(HALF, CTRL) _Pragma("unroll") for (int k = 0; k < (HALF); ++k) { \
      const float t1 = v[k] + dpp<CTRL>(v[k]), t2 = v[k + (HALF)] + dpp<CTRL>(v[k + (HALF)]); v[k] = (c & (HALF)) ? t2 : t1; }
    TR_STEP(8, XOR8) TR_STEP(4, XOR7) TR_STEP(2, XOR2) TR_STEP(1, XOR1)
#undef TR_STEP
    const float sc = rbf(rbf(v[0]) * 0.07216878364870322f);
    const float mw = xrow_max(row_max(sc));
    const float p = __expf(sc - mw);
    const float lw = wave_sum(p);
    pl = pack2(p, 0.f);
    if (lane == 0) { MISC[16 + w] = f2u(mw); MISC[24 + w] = f2u(lw); }
    asm volatile("s_waitcnt lgkmcnt(0)" ::: "memory");
    unsigned old = 0;
    if (lane == 0) old = __hip_atomic_fetch_add((LAS unsigned*)(L + L_MISC + 8), 1u, __ATOMIC_RELAXED, __HIP_MEMORY_SCOPE_WORKGROUP);
    if (__builtin_amdgcn_readfirstlane(old) == 7u) {
      const float m = u2f(MISC[16 + (c & 7)]), l = u2f(MISC[24 + (c & 7)]);
      const float M = row_max(m);
      const float Ls = row_sum(l * __expf(m - M)) * 0.5f;
      if (lane == 0) stsc(rws, WS_ML + (h * 8 + j8) * 16, u32x4{f2u(M), tag, f2u(Ls), tag});
    }
  }
  STAMP(6);
  SCHED();
  u32x4 BO[6];
  if (w < 5) {
    LAS unsigned char* pb = L + L_WO + g * 1408 + trq * 160 + trp * 8 + w * 32;
#pragma unroll
    for (int ks = 0; ks < 6; ++ks) BO[ks] = trfrag(pb + ks * 5632, 640);
  }
  float acc[24];
#pragma unroll
  for (int k = 0; k < 24; ++k) acc[k] = 0.f;
  const unsigned ph = pl << 16;
  float fsc = 0.f;
#define PV(a) { \
    const unsigned el = dppu<0x150 + 2 * (a)>(pl), ol = dppu<0x151 + 2 * (a)>(pl), eh = dppu<0x150 + 2 * (a)>(ph), oh = dppu<0x151 + 2 * (a)>(ph); \
    const unsigned ml = c < 8 ? el : ol, mh = c < 8 ? eh : oh; \
    _Pragma("unroll") for (int k = 0; k < 4; ++k) { \
      acc[2 * k] = dot2(V[3 * (a)][k], el, acc[2 * k]); acc[2 * k + 1] = dot2(V[3 * (a)][k], eh, acc[2 * k + 1]); \
      acc[8 + 2 * k] = dot2(V[3 * (a) + 1][k], ml, acc[8 + 2 * k]); acc[9 + 2 * k] = dot2(V[3 * (a) + 1][k], mh, acc[9 + 2 * k]); \
      acc[16 + 2 * k] = dot2(V[3 * (a) + 2][k], ol, acc[16 + 2 * k]); acc[17 + 2 * k] = dot2(V[3 * (a) + 2][k], oh, acc[17 + 2 * k]); } \
    SCHED(); }
  PV(0) PV(1) PV(2) PV(3) PV(4) PV(5)
  {
#pragma unroll 1
    for (unsigned spins = 0; MISC[2] < 8u && spins < (SPIN_CAP << 4); ++spins) __builtin_amdgcn_s_sleep(1);
    const float m = u2f(MISC[16 + (c >> 1)]);
    fsc = __expf(m - row_max(m));
  }
  SCHED();
  PV(6) PV(7)
#undef PV
  STAMP(7);
  {
    float u[12], z[6];
#pragma unroll
    for (int k = 0; k < 12; ++k) { auto r = __builtin_amdgcn_permlane32_swap(f2u(acc[k]), f2u(acc[k + 12]), false, false); u[k] = u2f(r[0]) + u2f(r[1]); }
#pragma unroll
    for (int k = 0; k < 6; ++k) { auto r = __builtin_amdgcn_permlane16_swap(f2u(u[k]), f2u(u[k + 6]), false, false); z[k] = u2f(r[0]) + u2f(r[1]); }
    LAS float* op = (LAS float*)(L + L_OP + w * 1552 + g * 384 + c * 4);
#pragma unroll
    for (int k = 0; k < 6; ++k) op[k * 16] = z[k];
  }
  wg_barrier();
  {
    float ov[6];
#pragma unroll
    for (int k = 0; k < 6; ++k) {
      const int d = 24 * w + 6 * g + k, ck = 24 * (c & 1) + (d >> 3);
      ov[k] = *(LAS float*)(L + L_OP + (c >> 1) * 1552 + ((ck >> 4) * 8 + (d & 7)) * 64 + (ck & 15) * 4) * fsc;
    }
#pragma unroll
    for (int k = 0; k < 6; ++k) ov[k] = row_sum(ov[k]);
    const float lo = c == 0 ? ov[0] : c == 1 ? ov[2] : ov[4], hi = c == 0 ? ov[1] : c == 1 ? ov[3] : ov[5];
    if (c < 3) stsc(rws, WS_OG + ((h * 8 + j8) * 96 + 12 * w + 3 * g + c) * 16, u32x4{f2u(lo), tag, f2u(hi), tag});
  }
  if ((PROBE == 3 || PROBE == 4) && bid < 8 && w == 7) {
    const unsigned long long t0 = __builtin_amdgcn_s_memrealtime();
    asm volatile("buffer_wbl2 sc1\n\ts_waitcnt vmcnt(0)" ::: "memory");
    wbd = (unsigned)(__builtin_amdgcn_s_memrealtime() - t0);
  }
  if (w < 6) {
    const int ob = tid / 96, ou = tid % 96;
    const int o0 = WS_OG + ((h * 8 + 2 * ob) * 96 + ou) * 16, m0 = WS_ML + (h * 8 + 2 * ob) * 16;
    u32x4 t0, t1, ma, mb;
#pragma unroll 1
    for (unsigned spins = 0; spins < SPIN_CAP; ++spins) {
      MEMBAR();
      t0 = ldsc(rws, o0); t1 = ldsc(rws, o0 + 1536); ma = ldsc(rws, m0); mb = ldsc(rws, m0 + 16);
      if (all0(tag2(t0, tag) | tag2(t1, tag) | tag2(ma, tag) | tag2(mb, tag))) break;
      __builtin_amdgcn_s_sleep(1);
    }
    const float M = fmaxf(u2f(ma.x), u2f(mb.x)), e0 = __expf(u2f(ma.x) - M), e1 = __expf(u2f(mb.x) - M);
    const float inv = 1.f / (u2f(ma.z) * e0 + u2f(mb.z) * e1), w0 = e0 * inv, w1 = e1 * inv;
    *(LAS unsigned*)(L + L_OA + ob * 448 + ou * 4) = pack2(u2f(t0.x) * w0 + u2f(t1.x) * w1, u2f(t0.z) * w0 + u2f(t1.z) * w1);
  }
  wg_barrier();
  STAMP(9);
  if (w < 5) {
    f32x4 y = {0, 0, 0, 0};
    LAS unsigned char* pa = L + L_OA + (c & 3) * 448 + g * 16;
#pragma unroll
    for (int ks = 0; ks < 6; ++ks) y = mfma32(*(LAS u32x4*)(pa + ks * 64), BO[ks], y);
    stsc8(rws, WS_YG + ((g * 40 + 5 * j8 + w) * 32 + h) * 128 + c * 8, u32x2{f2u(sel4(y, g)), tag});
  }
  STAMP(10);
  if (isRed) {
    float x1a = 0.f, x1b = 0.f; unsigned gb2 = 0;
    if (w == 0) {
      u32x4 t[4]; u32x2 ga, gb;
      const int yo = WS_YG + (rb * 40 + rt) * 4096 + lane * 16, mo = WS_MG + (rb * 1280 + 8 * rt + (lane & 7)) * 8;
#pragma unroll 1
      for (unsigned spins = 0; spins < SPIN_CAP; ++spins) {
        MEMBAR();
#pragma unroll
        for (int k = 0; k < 4; ++k) t[k] = ldsc(rws, yo + k * 1024);
        ga = ldsc8(rws, mo); gb = ldsc8(rws, mo + 960 * 8);
        if (all0(tag2(t[0], tag) | tag2(t[1], tag) | tag2(t[2], tag) | tag2(t[3], tag) | (ga.y ^ tag) | (gb.y ^ tag))) break;
        __builtin_amdgcn_s_sleep(1);
      }
      float ya = (u2f(t[0].x) + u2f(t[1].x)) + (u2f(t[2].x) + u2f(t[3].x)), yb = (u2f(t[0].z) + u2f(t[1].z)) + (u2f(t[2].z) + u2f(t[3].z));
      ya += dpp<XOR8>(ya); yb += dpp<XOR8>(yb);
      ya = xrow_sum(ya); yb = xrow_sum(yb);
      x1a = rbf(bflo(xres) + rbf(bflo(ga.x) * rbf(ya))); x1b = rbf(bfhi(xres) + rbf(bfhi(ga.x) * rbf(yb)));
      gb2 = gb.x;
      if (lane < 8) stsc8(rws, WS_XG + (rb * 320 + 8 * rt + lane) * 8, u32x2{pack2(x1a, x1b), tag});
    }
    STAMP(11);
    if (w < 6) {
      u32x4 t0, t1;
      const int po = WS_PG + (rb * 40 + rt) * 12288 + tid * 16;
#pragma unroll 1
      for (unsigned spins = 0; spins < SPIN_CAP; ++spins) {
        MEMBAR();
        t0 = ldsc(rws, po); t1 = ldsc(rws, po + 6144);
        if (all0(tag2(t0, tag) | tag2(t1, tag))) break;
        __builtin_amdgcn_s_sleep(2);
      }
      float ya = u2f(t0.x) + u2f(t1.x), yb = u2f(t0.z) + u2f(t1.z);
      ya += dpp<XOR8>(ya); yb += dpp<XOR8>(yb);
      ya = xrow_sum(ya); yb = xrow_sum(yb);
      if (lane < 8) *(LAS u32x2*)(L + L_RED + w * 64 + lane * 8) = u32x2{f2u(ya), f2u(yb)};
    }
    wg_barrier();
    if (tid < 8) {
      float ya = 0.f, yb = 0.f;
#pragma unroll
      for (int k = 0; k < 6; ++k) { const u32x2 r = *(LAS u32x2*)(L + L_RED + k * 64 + lane * 8); ya += u2f(r.x); yb += u2f(r.y); }
      ((unsigned*)out)[rb * 320 + 8 * rt + lane] = pack2(x1a + rbf(bflo(gb2) * rbf(ya)), x1b + rbf(bfhi(gb2) * rbf(yb)));
      if (bid == 0 && tid == 0) __hip_atomic_store((GAS unsigned*)(ws + WS_EPOCH), epoch + 1u, __ATOMIC_RELAXED, __HIP_MEMORY_SCOPE_AGENT);
    }
    if ((PROBE == 2 || PROBE == 4) && bid == 0 && w == 7) {
      const unsigned long long te = __builtin_amdgcn_s_memrealtime() + 4ull * wbd;
      while (__builtin_amdgcn_s_memrealtime() < te) __builtin_amdgcn_s_sleep(1);
    }
  } else {
    u32x4 BG[5]; u32x2 BD[5];
    {
      LAS unsigned char* pb = L + L_RL + (w >> 1) * (5 * 2176) + g * 544 + trq * 64 + trp * 8 + (w & 1) * 32;
#pragma unroll
      for (int ks = 0; ks < 5; ++ks) BG[ks] = trfrag(pb + ks * 2176, 256);
      LAS unsigned char* pd = L + L_WD + (4 * g + trq) * 1312 + trp * 8 + w * 160;
#pragma unroll
      for (int t = 0; t < 5; ++t) BD[t] = trread(pd + t * 32);
    }
    if (w < 4) {
      u32x4 sb[3], cb[3], t[3];
      int oo[3];
#pragma unroll
      for (int k = 0; k < 3; ++k) oo[k] = min(lane + 64 * k, 159) * 16;
#pragma unroll 1
      for (unsigned spins = 0; spins < SPIN_CAP; ++spins) {
        MEMBAR();
        unsigned bad = 0;
#pragma unroll
        for (int k = 0; k < 3; ++k) { sb[k] = ldsc(rws, WS_MG + w * 10240 + 2560 + oo[k]); cb[k] = ldsc(rws, WS_MG + w * 10240 + 5120 + oo[k]); }
#pragma unroll
        for (int k = 0; k < 3; ++k) bad |= tag2(sb[k], tag) | tag2(cb[k], tag);
        if (all0(bad)) break;
        __builtin_amdgcn_s_sleep(2);
      }
#pragma unroll 1
      for (unsigned spins = 0; spins < SPIN_CAP; ++spins) {
        MEMBAR();
#pragma unroll
        for (int k = 0; k < 3; ++k) t[k] = ldsc(rws, WS_XG + w * 2560 + oo[k]);
        if (all0(tag2(t[0], tag) | tag2(t[1], tag) | tag2(t[2], tag))) break;
        __builtin_amdgcn_s_sleep(1);
      }
      STAMP(11);
      float xv[12];
#pragma unroll
      for (int k = 0; k < 3; ++k) { xv[4 * k] = bflo(t[k].x); xv[4 * k + 1] = bfhi(t[k].x); xv[4 * k + 2] = bflo(t[k].z); xv[4 * k + 3] = bfhi(t[k].z); }
      const bool l3 = lane < 32;
      float sm = ((xv[0] + xv[1]) + (xv[2] + xv[3])) + ((xv[4] + xv[5]) + (xv[6] + xv[7]));
      sm += l3 ? (xv[8] + xv[9]) + (xv[10] + xv[11]) : 0.f;
      const float mean = wave_sum(sm) * (1.f / D);
      float sv = 0.f, sv3 = 0.f;
#pragma unroll
      for (int k = 0; k < 8; ++k) { const float dlt = xv[k] - mean; sv = fmaf(dlt, dlt, sv); }
#pragma unroll
      for (int k = 8; k < 12; ++k) { const float dlt = xv[k] - mean; sv3 = fmaf(dlt, dlt, sv3); }
      sv += l3 ? sv3 : 0.f;
      const float var = wave_sum(sv) * (1.f / D);
      const float mb = rbf(mean), den = rbf(sqrtf(rbf(rbf(var) + 1e-5f)));
#pragma unroll
      for (int k = 0; k < 3; ++k) {
        const float s0 = bflo(sb[k].x), s1 = bfhi(sb[k].x), s2 = bflo(sb[k].z), s3 = bfhi(sb[k].z);
        const float c0 = bflo(cb[k].x), c1 = bfhi(cb[k].x), c2 = bflo(cb[k].z), c3 = bfhi(cb[k].z);
        const float h0 = rbf(rbf(rbf(xv[4 * k] - mb) / den) * rbf(1.f + c0)) + s0, h1 = rbf(rbf(rbf(xv[4 * k + 1] - mb) / den) * rbf(1.f + c1)) + s1;
        const float h2 = rbf(rbf(rbf(xv[4 * k + 2] - mb) / den) * rbf(1.f + c2)) + s2, h3 = rbf(rbf(rbf(xv[4 * k + 3] - mb) / den) * rbf(1.f + c3)) + s3;
        *(LAS u32x2*)(L + L_A + w * 1344 + (oo[k] >> 1)) = u32x2{pack2(h0, h1), pack2(h2, h3)};
      }
    }
    wg_barrier();
    {
      f32x4 a = {0, 0, 0, 0};
      LAS unsigned char* pa = L + L_A + (c & 3) * 1344 + (w >> 1) * 320 + g * 16;
#pragma unroll
      for (int ks = 0; ks < 5; ++ks) a = mfma32(*(LAS u32x4*)(pa + ks * 64), BG[ks], a);
      *(LAS float*)(L + L_RED + w * 256 + lane * 4) = sel4(a, g);
    }
    wg_barrier();
    {
      f32x4 gs = {0, 0, 0, 0}, us = {0, 0, 0, 0};
#pragma unroll
      for (int k = 0; k < 4; ++k) {
        gs += *(LAS f32x4*)(L + L_RED + (2 * k) * 256 + (c & 3) * 64 + g * 16);
        us += *(LAS f32x4*)(L + L_RED + (2 * k + 1) * 256 + (c & 3) * 64 + g * 16);
      }
      float hh[4];
#pragma unroll
      for (int k = 0; k < 4; ++k) { const float gv = rbf(gs[k]); hh[k] = gv * (gv > 0.f ? 1.f : 0.f) * rbf(us[k]); }
      const u32x2 af = {pack2(hh[0], hh[1]), pack2(hh[2], hh[3])};
#pragma unroll
      for (int t = 0; t < 5; ++t) {
        f32x4 y = {0, 0, 0, 0};
        y = __builtin_amdgcn_mfma_f32_16x16x16bf16_1k(__builtin_bit_cast(s16x4, af), __builtin_bit_cast(s16x4, BD[t]), y, 0, 0, 0);
        stsc8(rws, WS_PG + ((g * 40 + 5 * w + t) * 96 + mid) * 128 + c * 8, u32x2{f2u(sel4(y, g)), tag});
      }
    }
    STAMP(12);
  }
}

extern "C" void kernel_launch(void* const* d_in, const int* in_sizes, int n_in, void* d_out, int out_size, void* d_ws, size_t ws_size, hipStream_t stream) {
  fused_block<<<256, 512, 0, stream>>>((const u16*)d_in[0], (const u16*)d_in[1], (const u16*)d_in[2], (const u16*)d_in[3], (const u16*)d_in[4],
                                       (const u16*)d_in[6], (const u16*)d_in[7], (const u16*)d_in[8], (const u16*)d_in[9], (const u16*)d_in[10],
                                       (u16*)d_out, (unsigned char*)d_ws);
}
```

```cpp
#include <hip/hip_runtime.h>
#include <hip/hip_bf16.h>
#include <stdint.h>

#pragma clang fp contract(off)
typedef unsigned short u16;
typedef unsigned u32x4 __attribute__((ext_vector_type(4)));
typedef unsigned u32x2 __attribute__((ext_vector_type(2)));
typedef float f32x4 __attribute__((ext_vector_type(4)));
typedef float f32x2 __attribute__((ext_vector_type(2)));
typedef short s16x4 __attribute__((ext_vector_type(4)));
typedef __bf16 bf16x2 __attribute__((ext_vector_type(2)));
typedef __bf16 bf16x8 __attribute__((ext_vector_type(8)));
#define LAS __attribute__((address_space(3)))
#define GAS __attribute__((address_space(1)))
typedef __amdgpu_buffer_rsrc_t rsrc_t;

constexpr int D = 640, NH = 32, DH = 192, SK = 4096, WIN = 1024, FF = 1536;
constexpr int AUX_SC1 = 16;
constexpr int W_S = 3, W_C = 12;
constexpr int K0 = 3, KSA = 6, KS1 = 21;
constexpr int CPS = 6, CPV = 3;
constexpr int NVA = 14, NVB = 18;
constexpr int H4_DELAY = 32;
constexpr int AUX_NT = 2;
constexpr unsigned SPIN_CAP = 1u << 16;

constexpr int WS_EPOCH = 0;
constexpr int WS_MG = 1024;
constexpr int WS_XG = WS_MG + 4 * 1920 * 8;
constexpr int WS_ML = WS_XG + 8 * 4 * 320 * 8;
constexpr int WS_OG = WS_ML + 256 * 128;
constexpr int WS_QG = WS_OG + 256 * 96 * 16;
constexpr int WS_YG = WS_QG + 16 * 3072 * 16;
constexpr int WS_PG = WS_YG + 4 * 40 * 32 * 128;
constexpr int WS_END = WS_PG + 4 * 40 * 96 * 128;

constexpr int L_MISC = 0;
constexpr int L_Q = 256;
constexpr int L_HA = L_Q + 384;
constexpr int L_OA = L_HA + 4 * 320;
constexpr int L_RED = L_OA + 4 * 448;
constexpr int L_A = L_RED + 8 * 256;
constexpr int L_OP = L_A + 4 * 1344;
constexpr int L_RL = L_OP + 8 * 1552;
constexpr int L_WQ = L_RL, L_WO = L_RL;
constexpr int L_GU = L_RL, L_WD = L_RL + 80 * 544;
constexpr int L_BYTES = L_RL + 160 * 544 + 20 * 128;
static_assert(L_BYTES <= 160 * 1024 && L_BYTES > 80 * 1024 && L_WD + 16 * 1312 <= L_BYTES, "one workgroup per CU");

__device__ __forceinline__ float bflo(unsigned u) { return __builtin_bit_cast(float, u << 16); }
__device__ __forceinline__ float bfhi(unsigned u) { return __builtin_bit_cast(float, u & 0xffff0000u); }
__device__ __forceinline__ unsigned pack2(float lo, float hi) { unsigned r; asm("v_cvt_pk_bf16_f32 %0, %1, %2" : "=v"(r) : "v"(lo), "v"(hi)); return r; }
__device__ __forceinline__ unsigned pack2c(float lo, float hi) { bf16x2 v = {(__bf16)lo, (__bf16)hi}; return __builtin_bit_cast(unsigned, v); }
__device__ __forceinline__ float rbf(float f) { return (float)(__bf16)f; }
__device__ __forceinline__ f32x2 unp2(unsigned r) { return f32x2{bflo(r), bfhi(r)}; }
__device__ __forceinline__ f32x2 rbf2(f32x2 v) { return unp2(pack2(v.x, v.y)); }
__device__ __forceinline__ unsigned f2u(float f) { return __builtin_bit_cast(unsigned, f); }
__device__ __forceinline__ float u2f(unsigned u) { return __builtin_bit_cast(float, u); }
__device__ __forceinline__ float dot2(unsigned a, unsigned b, float c) {
  return __builtin_amdgcn_fdot2_f32_bf16(__builtin_bit_cast(bf16x2, a), __builtin_bit_cast(bf16x2, b), c, false);
}
template <int CTRL> __device__ __forceinline__ float dpp(float x) { return u2f((unsigned)__builtin_amdgcn_mov_dpp((int)f2u(x), CTRL, 0xf, 0xf, true)); }
template <int CTRL> __device__ __forceinline__ unsigned dppu(unsigned x) { return (unsigned)__builtin_amdgcn_mov_dpp((int)x, CTRL, 0xf, 0xf, true); }
constexpr int XOR1 = 0xB1, XOR2 = 0x4E, XOR7 = 0x141, XOR8 = 0x128, ROR4 = 0x124;
__device__ __forceinline__ float row_sum(float v) { v += dpp<XOR1>(v); v += dpp<XOR2>(v); v += dpp<XOR7>(v); v += dpp<XOR8>(v); return v; }
__device__ __forceinline__ float row_max(float v) { v = fmaxf(v, dpp<XOR1>(v)); v = fmaxf(v, dpp<XOR2>(v)); v = fmaxf(v, dpp<XOR7>(v)); v = fmaxf(v, dpp<XOR8>(v)); return v; }
__device__ __forceinline__ float xrow_sum(float x) {
  auto s = __builtin_amdgcn_permlane16_swap(f2u(x), f2u(x), false, false);
  x = u2f(s[0]) + u2f(s[1]);
  auto t = __builtin_amdgcn_permlane32_swap(f2u(x), f2u(x), false, false);
  return u2f(t[0]) + u2f(t[1]);
}
__device__ __forceinline__ float xrow_max(float x) {
  auto s = __builtin_amdgcn_permlane16_swap(f2u(x), f2u(x), false, false);
  x = fmaxf(u2f(s[0]), u2f(s[1]));
  auto t = __builtin_amdgcn_permlane32_swap(f2u(x), f2u(x), false, false);
  return fmaxf(u2f(t[0]), u2f(t[1]));
}
__device__ __forceinline__ float wave_sum(float v) { return xrow_sum(row_sum(v)); }
__device__ __forceinline__ float ln_den(float var) { return rbf(__builtin_amdgcn_sqrtf(rbf(rbf(var) + 1e-5f))); }
__device__ __forceinline__ rsrc_t mkrs(const void* p, unsigned bytes) { return __builtin_amdgcn_make_buffer_rsrc((void*)p, 0, bytes, 0x00020000); }
template <int AUX> __device__ __forceinline__ u32x4 ldx(rsrc_t rs, int voff, int soff) { return __builtin_amdgcn_raw_buffer_load_b128(rs, voff, soff, AUX); }
#define ldnt ldx<AUX_NT>
__device__ __forceinline__ u32x4 ldsc(rsrc_t rs, int voff) { return __builtin_amdgcn_raw_buffer_load_b128(rs, voff, 0, AUX_SC1); }
__device__ __forceinline__ void stsc(rsrc_t rs, int voff, u32x4 v) { __builtin_amdgcn_raw_buffer_store_b128(v, rs, voff, 0, AUX_SC1); }
__device__ __forceinline__ void pace(int pm) {
  asm volatile("s_cmp_gt_u32 %0, 7\n\ts_cbranch_scc1 1f\n\ts_waitcnt vmcnt(%2)\n\ts_cmp_gt_u32 %0, 3\n\ts_cbranch_scc1 1f\n\ts_waitcnt vmcnt(%1)\n1:"
               ::"s"(pm), "i"(W_S - 1), "i"(W_C - 1) : "scc", "memory");
}
#define SCHED() __builtin_amdgcn_sched_barrier(0)
#define MEMBAR() asm volatile("" ::: "memory")
#define PIN(v) asm volatile("" : "+v"(v))
__device__ __forceinline__ void wg_barrier() { asm volatile("s_waitcnt lgkmcnt(0)\n\ts_barrier" ::: "memory"); }
__device__ __forceinline__ f32x4 mfma32(u32x4 a, u32x4 b, f32x4 c) {
  return __builtin_amdgcn_mfma_f32_16x16x32_bf16(__builtin_bit_cast(bf16x8, a), __builtin_bit_cast(bf16x8, b), c, 0, 0, 0);
}
__device__ __forceinline__ u32x2 trread(LAS unsigned char* p) { return __builtin_bit_cast(u32x2, __builtin_amdgcn_ds_read_tr16_b64_v4i16((LAS s16x4*)p)); }
__device__ __forceinline__ u32x4 trfrag(LAS unsigned char* p, int step) {
  const u32x2 lo = trread(p), hi = trread(p + step);
  return u32x4{lo.x, lo.y, hi.x, hi.y};
}
__device__ __forceinline__ unsigned tag2(u32x4 v, unsigned tag) { return (v.y ^ tag) | (v.w ^ tag); }
__device__ __forceinline__ bool all0(unsigned bad) { return __builtin_amdgcn_ballot_w64(bad != 0u) == 0ull; }
__device__ __forceinline__ void lds_arrive(LAS unsigned char* p, int lane) {
  asm volatile("s_waitcnt lgkmcnt(0)" ::: "memory");
  if (lane == 0) __hip_atomic_fetch_add((LAS unsigned*)p, 1u, __ATOMIC_RELAXED, __HIP_MEMORY_SCOPE_WORKGROUP);
}
__device__ __forceinline__ void lds_wait(volatile LAS unsigned* p, unsigned want) {
#pragma unroll 1
  for (unsigned spins = 0; *p < want && spins < (SPIN_CAP << 4); ++spins) __builtin_amdgcn_s_sleep(1);
  MEMBAR();
}
template <int N, int SLEEP, bool ANY = false>
__device__ __forceinline__ void sweep(rsrc_t rws, const int (&off)[N], u32x4 (&t)[N], unsigned tag, bool probe = true) {
  unsigned miss = (1u << N) - 1u;
  bool act = probe;
#pragma unroll
  for (int k = 0; k < N; ++k) t[k] = u32x4{0u, ~tag, 0u, ~tag};
#pragma unroll 1
  for (unsigned spins = 0; spins < SPIN_CAP; ++spins) {
    MEMBAR();
    const unsigned go = act ? miss : 0u;
#pragma unroll
    for (int k = 0; k < N; ++k) if (go & (1u << k)) t[k] = ldsc(rws, off[k]);
#pragma unroll
    for (int k = 0; k < N; ++k) if (tag2(t[k], tag) == 0u) miss &= ~(1u << k);
    if (all0(miss)) break;
    if (ANY ? !all0(miss != (1u << N) - 1u) : all0(act ? miss : 0u)) act = true;
    else if (SLEEP) __builtin_amdgcn_s_sleep(SLEEP);
  }
}

template <int N> __device__ __forceinline__ bool poll_step(rsrc_t rws, const int (&off)[N], u32x4 (&t)[N], unsigned tag) {
  unsigned miss = 0u;
#pragma unroll
  for (int k = 0; k < N; ++k) miss |= (tag2(t[k], tag) != 0u ? 1u : 0u) << k;
  if (all0(miss)) return true;
  MEMBAR();
#pragma unroll
  for (int k = 0; k < N; ++k) if (miss & (1u << k)) t[k] = ldsc(rws, off[k]);
  return false;
}
template <int N, int SLEEP> __device__ __forceinline__ void poll_end(rsrc_t rws, const int (&off)[N], u32x4 (&t)[N], unsigned tag) {
#pragma unroll 1
  for (unsigned spins = 0; spins < SPIN_CAP; ++spins) { if (poll_step<N>(rws, off, t, tag)) break; if (SLEEP) __builtin_amdgcn_s_sleep(SLEEP); }
}

__global__ void __launch_bounds__(512, 2)
fused_block(const u16* __restrict__ xin, const u16* __restrict__ kc, const u16* __restrict__ vc, const u16* __restrict__ wq,
            const u16* __restrict__ wo, const u16* __restrict__ wg, const u16* __restrict__ wu, const u16* __restrict__ wd,
            const u16* __restrict__ cin, const u16* __restrict__ wmod, u16* __restrict__ out, unsigned char* ws) {
  __shared__ __attribute__((aligned(16))) unsigned char smem[L_BYTES];
  LAS unsigned char* L = (LAS unsigned char*)smem;
  volatile LAS unsigned* MISC = (volatile LAS unsigned*)(L + L_MISC);
  const int tid = threadIdx.x, lane = tid & 63, w = __builtin_amdgcn_readfirstlane(tid >> 6), g = lane >> 4, c = lane & 15;
  const int bid = blockIdx.x, x = bid & 7, slot = bid >> 3, h = 4 * x + (slot >> 3), j8 = slot & 7, b = j8 >> 1, s = j8 & 1;
  const int mu = slot * 8 + x, qu = mu - 60, kq = qu & 3, qg = qu >> 2;
  const bool isRed = slot < 20, isMod = mu < 60, isSA = mu < 20, isRU = isMod && !isSA, isWq = qu >= 0 && qu < 96;
  const int rid = x * 20 + slot, rb = rid / 40, rt = rid % 40;
  const int ph = rid / 5, pf = rid % 5;
  const int mid = x * 12 + (slot - 20);
  constexpr int pm = 3;
  const int pmA = isSA ? 9 : pm;
#define PACEQ(p) do { if (__builtin_amdgcn_readfirstlane(MISC[1]) == 0) pace(p); } while (0)

  const rsrc_t rws = mkrs(ws, WS_END);
  const size_t kvbase = ((size_t)(b * NH + h) * SK + (SK - WIN) + s * 512 + w * 8) * DH;
  const rsrc_t rk = mkrs(kc + kvbase, (512 - 8 * w) * 384), rv = mkrs(vc + kvbase, (512 - 8 * w) * 384);
  const rsrc_t rm = mkrs(wmod, D * 6 * D * 2);
  const int kvo = g * 768 + c * 16;
  u32x4 K[24], V[24];
#define LDK(n) K[n] = ldnt(rk, kvo + ((n) % 3) * 256, ((n) / 3) * 24576)
#define LDV(n) V[n] = ldnt(rv, kvo + ((n) % 3) * 256, ((n) / 3) * 24576)
  const int trq = (c >> 2), trp = (c & 3);
  auto mod_stage = [&](const u32x4 (&SR)[10], u32x4 cv) {
    if (tid < 320) {
      u32x4 o;
#pragma unroll
      for (int k = 0; k < 4; ++k) { const float a = bflo(cv[k]), bb = bfhi(cv[k]); o[k] = pack2c(a / (1.f + __expf(-a)), bb / (1.f + __expf(-bb))); }
      *(LAS u32x4*)(L + L_A + (tid / 80) * 1344 + (tid % 80) * 16) = o;
    }
    const int row = tid >> 3, f = ((row >> 1) & 1) | (((row >> 3) & 1) << 1);
    LAS unsigned char* pw = L + L_RL + row * 128 + ((((tid & 7) >> 1) ^ f) << 5) + (tid & 1) * 16;
#pragma unroll
    for (int k = 0; k < 10; ++k) *(LAS u32x4*)(pw + k * 8192) = SR[k];
  };
  auto mod_tile = [&](int t, unsigned tag) {
    f32x4 a0 = {0, 0, 0, 0}, a1 = {0, 0, 0, 0};
    LAS unsigned char* pa = L + L_A + (c >> 2) * 1344 + g * 16;
    LAS unsigned char* pb = L + L_RL + (8 * g + trq) * 128 + ((t ^ ((trq >> 1) | ((g & 1) << 1))) << 5) + trp * 8;
#pragma unroll
    for (int ks = 0; ks < 20; ks += 2) {
      a0 = mfma32(*(LAS u32x4*)(pa + ks * 64), trfrag(pb + ks * 4096, 512), a0);
      a1 = mfma32(*(LAS u32x4*)(pa + ks * 64 + 64), trfrag(pb + ks * 4096 + 4096, 512), a1);
    }
    const float v0 = a0[0] + a1[0], n0 = dpp<XOR1>(v0);
    const unsigned pv = pack2c(v0, n0), pn = dppu<XOR2>(pv);
    if ((c & 3) == 0) stsc(rws, WS_MG + (g * 1920 + 32 * mu + 8 * t + (c >> 1)) * 8, u32x4{pv, tag, pn, tag});
  };

  unsigned epoch = 0, xres = 0;
  u32x2 xs[3];
  {
    u32x4 SR[10], cv = {0, 0, 0, 0};
    unsigned xr[5] = {0, 0, 0, 0, 0};
    epoch = __builtin_amdgcn_raw_buffer_load_b32(rws, (isMod || w < 2) ? WS_EPOCH : 0x7ffffff0, 0, AUX_SC1);
    SCHED();
    if (isRed) {
      if (isMod) {
        const int vo = (tid >> 3) * 7680 + 128 * mu + (tid & 7) * 16;
#pragma unroll
        for (int k = 0; k < 10; ++k) SR[k] = ldnt(rm, vo, k * 64 * 7680);
        if (tid < 320) cv = *(const u32x4*)(cin + tid * 8);
      }
      if (isWq) {
        const rsrc_t rq = mkrs(wq + (size_t)(160 * kq) * (NH * DH) + 256 * qg, 160 * NH * DH * 2 - 512 * qg);
#pragma unroll
        for (int k = 0; k < 10; ++k) SR[k] = ldnt(rq, (tid >> 5) * 12288 + (tid & 31) * 16, k * 16 * 12288);
        if (w >= 4) {
#pragma unroll
          for (int k = 0; k < 5; ++k) xr[k] = ((const unsigned*)xin)[(w - 4) * 320 + lane + 64 * k];
        }
        if (w == 0) {
#pragma unroll
          for (int k = 0; k < 3; ++k) { const int n = min(lane + 64 * k, 159); xs[k] = *(const u32x2*)(xin + (n / 40) * D + 160 * kq + (n % 40) * 4); }
        }
      }
      if (w == 0) xres = ((const unsigned*)xin)[rb * 320 + rt * 8 + (lane & 7)];
    } else {
      const rsrc_t rg = mkrs(w < 4 ? wg : wu, D * FF * 2);
      const int vo = (160 * (mid & 3) + ((tid & 255) >> 3)) * 3072 + (mid >> 2) * 128 + (tid & 7) * 16;
#pragma unroll
      for (int k = 0; k < 5; ++k) SR[k] = ldx<0>(rg, vo, k * 32 * 3072);
    }
    SCHED();
#pragma unroll
    for (int n = 0; n < K0; ++n) LDK(n);
    SCHED();
    if (tid < 40) MISC[tid] = 0u;
    wg_barrier();
#pragma unroll
    for (int n = K0; n < KSA; ++n) { pace(pmA); LDK(n); SCHED(); }
    if (isMod) mod_stage(SR, cv);
    if (isSA) {
      wg_barrier();
      if (w < 4) { mod_tile(w, (epoch + 1u) * 0x9E3779B1u); lds_arrive(L + L_MISC + 132, lane); }
    }
    if (isWq) {
      if (w >= 4) {
        float sm = 0.f;
#pragma unroll
        for (int k = 0; k < 5; ++k) sm += bflo(xr[k]) + bfhi(xr[k]);
        const float mean = wave_sum(sm) * (1.f / D);
        float sv = 0.f;
#pragma unroll
        for (int k = 0; k < 5; ++k) { const float a = bflo(xr[k]) - mean, bb = bfhi(xr[k]) - mean; sv += a * a + bb * bb; }
        const float var = wave_sum(sv) * (1.f / D);
        if (lane == 0) { MISC[4 + 2 * (w - 4)] = f2u(rbf(mean)); MISC[5 + 2 * (w - 4)] = f2u(__builtin_amdgcn_rcpf(ln_den(var))); }
      }
      LAS unsigned char* pw = L + L_WQ + (tid >> 5) * 544 + (tid >> 8) * 128 + (tid & 31) * 16;
#pragma unroll
      for (int k = 0; k < 10; ++k) *(LAS u32x4*)(pw + k * (16 * 544 + 256)) = SR[k];
    } else if (!isRed && ((tid & 7) >> 1) == (mid & 3)) {
#pragma unroll
      for (int k = 0; k < 5; ++k) { const int row = 160 * (mid & 3) + ((tid & 255) >> 3) + 32 * k; *(LAS u32x4*)(L + L_GU + row * 64 + (row >> 3) * 32 + (w >> 2) * 32 + (tid & 1) * 16) = SR[k]; }
    }
    lds_arrive(L + L_MISC + 12, lane);
  }
  SCHED();
  if (isRU && w >= 4) { lds_wait(MISC + 3, 8u); mod_tile(w - 4, (epoch + 1u) * 0x9E3779B1u); lds_arrive(L + L_MISC + 132, lane); }
  SCHED();
  const unsigned tagw = (epoch + 1u) * 0x9E3779B1u;
  u32x4 tp[6];
#pragma unroll
  for (int k = 0; k < 6; ++k) tp[k] = u32x4{0u, ~tagw, 0u, ~tagw};
  int off1[6];
#pragma unroll
  for (int k = 0; k < 3; ++k) { const int n = min(lane + 64 * k, 159); off1[k] = WS_MG + (n / 40) * 15360 + kq * 640 + (n % 40) * 16; off1[3 + k] = off1[k] + 2560; }
  const bool svc1 = isWq && w == 0;
  int pw = svc1 ? 7 : pm;
  const int pq = (w == 1 || svc1) ? 7 : pm;
#pragma unroll
  for (int n = KSA; n < KS1; ++n) {
    if ((n - KSA) % CPS == 0 && svc1 && pw != 9) { if (poll_step<6>(rws, off1, tp, tagw)) pw = 9; }
    pace(__builtin_amdgcn_readfirstlane(pw)); LDK(n); SCHED();
  }
  if (svc1) {
    const unsigned tag = tagw;
    poll_end<6, 4>(rws, off1, tp, tag);
    lds_wait(MISC + 3, 8u);
#pragma unroll
    for (int k = 0; k < 3; ++k) {
      const int n = min(lane + 64 * k, 159);
      const float mb = u2f(MISC[4 + 2 * (n / 40)]), rd = u2f(MISC[5 + 2 * (n / 40)]);
      const f32x2 h0 = rbf2(rbf2(rbf2(unp2(xs[k].x) - mb) * rd) * rbf2(1.f + unp2(tp[3 + k].x))) + unp2(tp[k].x);
      const f32x2 h1 = rbf2(rbf2(rbf2(unp2(xs[k].y) - mb) * rd) * rbf2(1.f + unp2(tp[3 + k].z))) + unp2(tp[k].z);
      *(LAS u32x2*)(L + L_HA + (n / 40) * 320 + (n % 40) * 8) = u32x2{pack2(h0.x, h0.y), pack2(h1.x, h1.y)};
    }
    u32x4 af[5];
    LAS unsigned char* pa = L + L_HA + (c >> 2) * 320 + g * 16;
#pragma unroll
    for (int ks = 0; ks < 5; ++ks) af[ks] = *(LAS u32x4*)(pa + ks * 64);
    LAS unsigned char* pb = L + L_WQ + g * (8 * 544 + 128) + trq * 544 + trp * 8;
    const int qo = WS_QG + ((g * 4 + kq) * 3072 + 128 * qg + c) * 16;
#pragma unroll
    for (int t2 = 0; t2 < 8; ++t2) {
      f32x4 a0 = {0, 0, 0, 0}, a1 = {0, 0, 0, 0};
#pragma unroll
      for (int ks = 0; ks < 5; ++ks) {
        a0 = mfma32(af[ks], trfrag(pb + ks * (32 * 544 + 512) + t2 * 64, 4 * 544), a0);
        a1 = mfma32(af[ks], trfrag(pb + ks * (32 * 544 + 512) + t2 * 64 + 32, 4 * 544), a1);
      }
      stsc(rws, qo + t2 * 256, u32x4{f2u(a0[0]), tag, f2u(a1[0]), tag});
    }
    asm volatile("s_waitcnt lgkmcnt(0)" ::: "memory");
    MISC[33] = 4u;
  }
  SCHED();
  const int o1 = WS_QG + (b * 4 * 3072 + 96 * h + lane) * 16, o2 = o1 + 1024 + (lane >> 5) * (2 * 49152 - 512);
  const int off2[6] = {o1, o1 + 49152, o1 + 2 * 49152, o1 + 3 * 49152, o2, o2 + 49152};
  bool qd = false;
  auto q_finish = [&]() {
    const u32x4 (&t)[6] = tp;
    const float qa = (u2f(t[0].x) + u2f(t[1].x)) + (u2f(t[2].x) + u2f(t[3].x)), qb = (u2f(t[0].z) + u2f(t[1].z)) + (u2f(t[2].z) + u2f(t[3].z));
    float qc = u2f(t[4].x) + u2f(t[5].x), qd2 = u2f(t[4].z) + u2f(t[5].z);
    { auto r = __builtin_amdgcn_permlane32_swap(f2u(qc), f2u(qc), false, false); qc = u2f(r[0]) + u2f(r[1]); }
    { auto r = __builtin_amdgcn_permlane32_swap(f2u(qd2), f2u(qd2), false, false); qd2 = u2f(r[0]) + u2f(r[1]); }
    LAS u16* Q = (LAS u16*)(L + L_Q);
    const unsigned p1 = pack2(qa, qb), p2 = pack2(qc, qd2);
    Q[32 * g + c] = (u16)p1; Q[32 * g + c + 16] = (u16)(p1 >> 16);
    if (lane >= 32) { Q[64 + 32 * g + c] = (u16)p2; Q[64 + 32 * g + c + 16] = (u16)(p2 >> 16); }
    MISC[0] = tagw;
    lds_arrive(L + L_MISC + 4, lane);
  };
#define CPQ() do { if (w == 1 && !qd) { if (poll_step<6>(rws, off2, tp, tagw)) { qd = true; q_finish(); } } } while (0)
#pragma unroll
  for (int n = KS1; n < 24; ++n) { PACEQ(pq); LDK(n); SCHED(); }
  {
    u32x4 SO[6];
    if (isRed) {
      const rsrc_t ro = mkrs(wo + (size_t)(DH * ph) * D + 128 * pf, DH * D * 2 - 256 * pf);
#pragma unroll
      for (int k = 0; k < 6; ++k) { PACEQ(pq); SO[k] = ldnt(ro, (tid >> 4) * 1280 + (tid & 15) * 16, k * 32 * 1280); SCHED(); }
    }
    SCHED();
#pragma unroll
    for (int n = 0; n < 6; ++n) { if (n % CPV == 0) CPQ(); PACEQ(pq); LDV(n); SCHED(); }
    if (isRed) {
      if (isMod || isWq) lds_wait(MISC + 33, 4u);
      const int row = tid >> 4;
      LAS unsigned char* pw = L + L_WO + row * 288 + (row >> 3) * 128 + (tid & 15) * 16;
#pragma unroll
      for (int k = 0; k < 6; ++k) *(LAS u32x4*)(pw + k * (32 * 288 + 512)) = SO[k];
    }
    lds_arrive(L + L_MISC + 48, lane);
  }
  SCHED();
#pragma unroll
  for (int n = 6; n < NVA; ++n) { if (n % CPV == 0) CPQ(); PACEQ(pq); LDV(n); SCHED(); }
  if (w == 1 && !qd) { poll_end<6, 4>(rws, off2, tp, tagw); q_finish(); }
  lds_wait(MISC + 1, 1u);
  SCHED();
#pragma unroll
  for (int n = NVA; n < NVB; ++n) { LDV(n); SCHED(); }
  const unsigned tag = MISC[0];
  unsigned pl;
  {
    LAS unsigned char* pq = L + L_Q;
    const u32x4 q0 = *(LAS u32x4*)(pq + c * 16), q1 = *(LAS u32x4*)(pq + ((16 + c) % 24) * 16), q2 = *(LAS u32x4*)(pq + (8 + c) * 16);
    float v[16];
#pragma unroll
    for (int a = 0; a < 8; ++a) {
      float d1 = 0.f;
#pragma unroll
      for (int k = 0; k < 4; ++k) d1 = dot2(K[3 * a + 1][k], q1[k], d1);
      float e = c < 8 ? d1 : 0.f, o = c < 8 ? 0.f : d1;
#pragma unroll
      for (int k = 0; k < 4; ++k) { e = dot2(K[3 * a][k], q0[k], e); o = dot2(K[3 * a + 2][k], q2[k], o); }
      v[2 * a] = e; v[2 * a + 1] = o;
    }
#define TR_STEP(HALF, CTRL) _Pragma("unroll") for (int k = 0; k < (HALF); ++k) { \
      const float t1 = v[k] + dpp<CTRL>(v[k]), t2 = v[k + (HALF)] + dpp<CTRL>(v[k + (HALF)]); v[k] = (c & (HALF)) ? t2 : t1; }
    TR_STEP(8, XOR8) TR_STEP(4, XOR7) TR_STEP(2, XOR2) TR_STEP(1, XOR1)
#undef TR_STEP
    const float sc = rbf(rbf(v[0]) * 0.07216878364870322f);
    const float mw = xrow_max(row_max(sc)), p = __expf(sc - mw);
    const float lw = wave_sum(p);
    pl = pack2c(p, 0.f);
    if (lane == 0) { MISC[16 + w] = f2u(mw); MISC[24 + w] = f2u(lw); }
    lds_arrive(L + L_MISC + 8, lane);
  }
  SCHED();
#pragma unroll
  for (int n = NVB; n < 24; ++n) { LDV(n); SCHED(); }
  const int ck0 = 24 * (c & 1) + 4 * min(w, 5) + g;
  int opr = L_OP + (c >> 1) * 1552 + (ck0 >> 4) * 512 + (ck0 & 15) * 4;
  PIN(opr);
  int a_og = WS_OG + ((h * 8 + j8) * 96 + 16 * w + 4 * g + c) * 16;
  int a_h1 = WS_OG + ((ph * 8 + (w & 6)) * 96 + 48 * (w & 1) + min(lane, 47)) * 16, a_oa = L_OA + (w >> 1) * 448 + (48 * (w & 1) + min(lane, 47)) * 4;
  int a_yg = WS_YG + ((g * 40 + 8 * pf + w) * 32 + ph) * 128 + c * 8;
  int a_ml = WS_ML + (ph * 8 + (w & 6)) * 128;
  PIN(a_og); PIN(a_h1); PIN(a_oa); PIN(a_yg); PIN(a_ml);
  SCHED();
  const unsigned ph16 = pl << 16;
  float acc[24];
#pragma unroll
  for (int k = 0; k < 24; ++k) acc[k] = 0.f;
#define PINACC() _Pragma("unroll") for (int k = 0; k < 24; ++k) PIN(acc[k])
#define PV(a) { \
    const unsigned el = dppu<0x150 + 2 * (a)>(pl), ol = dppu<0x151 + 2 * (a)>(pl), eh = dppu<0x150 + 2 * (a)>(ph16), oh = dppu<0x151 + 2 * (a)>(ph16); \
    const unsigned ml = c < 8 ? el : ol, mh = c < 8 ? eh : oh; \
    _Pragma("unroll") for (int k = 0; k < 4; ++k) { \
      acc[2 * k] = dot2(V[3 * (a)][k], el, acc[2 * k]); acc[2 * k + 1] = dot2(V[3 * (a)][k], eh, acc[2 * k + 1]); \
      acc[8 + 2 * k] = dot2(V[3 * (a) + 1][k], ml, acc[8 + 2 * k]); acc[9 + 2 * k] = dot2(V[3 * (a) + 1][k], mh, acc[9 + 2 * k]); \
      acc[16 + 2 * k] = dot2(V[3 * (a) + 2][k], ol, acc[16 + 2 * k]); acc[17 + 2 * k] = dot2(V[3 * (a) + 2][k], oh, acc[17 + 2 * k]); } \
    SCHED(); }
  PV(0) PV(1) PV(2) PV(3) PV(4)
  PINACC();
  u32x4 BO[6];
  const int a_gg[2] = {WS_MG + (rb * 1920 + 640 + 8 * rt + (lane & 6)) * 8, WS_MG + (rb * 1920 + 1600 + 8 * rt + (lane & 6)) * 8};
  u32x4 gg[2];
  if (isRed) {
    lds_wait(MISC + 12, 8u);
    LAS unsigned char* pb = L + L_WO + g * (8 * 288 + 128) + trq * 288 + trp * 8 + w * 32;
#pragma unroll
    for (int ks = 0; ks < 6; ++ks) { BO[ks] = trfrag(pb + ks * (32 * 288 + 512), 4 * 288); PIN(BO[ks]); }
  }
  float fw;
  const int a_m2[2] = {a_ml, a_ml + 128};
  u32x4 tm[2];
  {
    lds_wait(MISC + 2, 8u);
    const float m = u2f(MISC[16 + (c & 7)]), l = u2f(MISC[24 + (c & 7)]);
    const float M = row_max(m);
    fw = __expf(u2f(MISC[16 + w]) - M);
    PIN(fw);
    if (w == 2) {
      const float Ls = row_sum(l * __expf(m - M)) * 0.5f;
      if (lane < 8) stsc(rws, WS_ML + (h * 8 + j8) * 128 + lane * 16, u32x4{f2u(M), tag, f2u(Ls), tag});
    }
  }
  MEMBAR();
  tm[0] = ldsc(rws, a_m2[0]); tm[1] = ldsc(rws, a_m2[1]); gg[0] = ldsc(rws, a_gg[0]); gg[1] = ldsc(rws, a_gg[1]);
  SCHED();
  PV(5) PV(6)
#undef PV
  LAS float* op = (LAS float*)(L + L_OP + w * 1552 + c * 4);
  {
    const unsigned el = dppu<0x15E>(pl), ol = dppu<0x15F>(pl), eh = dppu<0x15E>(ph16), oh = dppu<0x15F>(ph16);
    const unsigned ml = c < 8 ? el : ol, mh = c < 8 ? eh : oh;
#pragma unroll
    for (int k = 0; k < 4; ++k) { acc[2 * k] = dot2(V[21][k], el, acc[2 * k]); acc[2 * k + 1] = dot2(V[21][k], eh, acc[2 * k + 1]); }
#pragma unroll
    for (int k = 0; k < 4; ++k) { acc[8 + 2 * k] = dot2(V[22][k], ml, acc[8 + 2 * k]); acc[9 + 2 * k] = dot2(V[22][k], mh, acc[9 + 2 * k]); }
    float u[8], z[4];
#pragma unroll
    for (int k = 0; k < 8; ++k) { auto r = __builtin_amdgcn_permlane32_swap(f2u(acc[k]), f2u(acc[k + 8]), false, false); u[k] = u2f(r[0]) + u2f(r[1]); }
#pragma unroll
    for (int k = 0; k < 4; ++k) { auto r = __builtin_amdgcn_permlane16_swap(f2u(u[k]), f2u(u[k + 4]), false, false); z[k] = (u2f(r[0]) + u2f(r[1])) * fw; }
#pragma unroll
    for (int k = 0; k < 4; ++k) op[(4 * g + k) * 16] = z[k];
    SCHED();
#pragma unroll
    for (int k = 0; k < 4; ++k) { acc[16 + 2 * k] = dot2(V[23][k], ol, acc[16 + 2 * k]); acc[17 + 2 * k] = dot2(V[23][k], oh, acc[17 + 2 * k]); }
#pragma unroll
    for (int k = 0; k < 4; ++k) { auto r = __builtin_amdgcn_permlane32_swap(f2u(acc[16 + k]), f2u(acc[20 + k]), false, false); u[k] = u2f(r[0]) + u2f(r[1]); }
#pragma unroll
    for (int k = 0; k < 2; ++k) { auto r = __builtin_amdgcn_permlane16_swap(f2u(u[k]), f2u(u[k + 2]), false, false); z[k] = (u2f(r[0]) + u2f(r[1])) * fw; }
    op[(16 + 2 * g) * 16] = z[0]; op[(17 + 2 * g) * 16] = z[1];
  }
  wg_barrier();
  if (w < 6) {
    float ov[8];
#pragma unroll
    for (int k = 0; k < 8; ++k) ov[k] = *(LAS float*)(L + opr + k * 64);
#pragma unroll
    for (int k = 0; k < 4; ++k) { const float t1 = ov[k] + dpp<XOR2>(ov[k]), t2 = ov[k + 4] + dpp<XOR2>(ov[k + 4]); ov[k] = (c & 2) ? t2 : t1; }
#pragma unroll
    for (int k = 0; k < 2; ++k) { const float t1 = ov[k] + dpp<XOR1>(ov[k]), t2 = ov[k + 2] + dpp<XOR1>(ov[k + 2]); ov[k] = (c & 1) ? t2 : t1; }
#pragma unroll
    for (int k = 0; k < 2; ++k) { ov[k] += dpp<ROR4>(ov[k]); ov[k] += dpp<XOR8>(ov[k]); }
    if (c < 4) stsc(rws, a_og, u32x4{f2u(ov[0]), tag, f2u(ov[1]), tag});
  }
  if (isRed) {
    f32x2 ga = {0.f, 0.f}; unsigned gb2 = 0;
    {
      const int off[2] = {a_h1, a_h1 + 1536};
      u32x4 t[2] = {u32x4{0u, ~tag, 0u, ~tag}, u32x4{0u, ~tag, 0u, ~tag}};
      poll_step<2>(rws, off, t, tag);
      poll_end<2, 1>(rws, a_m2, tm, tag);
      const float M = fmaxf(u2f(tm[0].x), u2f(tm[1].x)), e0 = __expf(u2f(tm[0].x) - M), e1 = __expf(u2f(tm[1].x) - M);
      const float inv = __builtin_amdgcn_rcpf(u2f(tm[0].z) * e0 + u2f(tm[1].z) * e1);
      float w0 = e0 * inv, w1 = e1 * inv;
      PIN(w0); PIN(w1);
      if (w == 0) {
        poll_end<2, 1>(rws, a_gg, gg, tag);
        ga = unp2((lane & 1) ? gg[0].z : gg[0].x); gb2 = (lane & 1) ? gg[1].z : gg[1].x;
        PIN(ga); PIN(gb2);
      }
      poll_end<2, 1>(rws, off, t, tag);
      *(LAS unsigned*)(L + a_oa) = pack2(u2f(t[0].x) * w0 + u2f(t[1].x) * w1, u2f(t[0].z) * w0 + u2f(t[1].z) * w1);
    }
    wg_barrier();
    {
      u32x4 af[6];
      LAS unsigned char* pa = L + L_OA + (c >> 2) * 448 + g * 16;
#pragma unroll
      for (int ks = 0; ks < 6; ++ks) af[ks] = *(LAS u32x4*)(pa + ks * 64);
      f32x4 y0 = {0, 0, 0, 0}, y1 = {0, 0, 0, 0};
#pragma unroll
      for (int ks = 0; ks < 6; ks += 2) { y0 = mfma32(af[ks], BO[ks], y0); y1 = mfma32(af[ks + 1], BO[ks + 1], y1); }
      const float yv = y0[0] + y1[0], yn = dpp<XOR1>(yv);
      if ((c & 1) == 0) stsc(rws, a_yg, u32x4{f2u(yv), tag, f2u(yn), tag});
    }
    f32x2 x1 = {0.f, 0.f};
    if (w == 0) {
      u32x4 t[4];
      f32x2 xr2 = unp2(xres);
      const int yo = WS_YG + (rb * 40 + rt) * 4096 + lane * 16;
      int a_xg = WS_XG + ((lane >> 3) * 1280 + rb * 320 + 8 * rt + (lane & 6)) * 8;
      PIN(xr2); PIN(a_xg);
      const int off[4] = {yo, yo + 1024, yo + 2048, yo + 3072};
      sweep<4, 1>(rws, off, t, tag);
      float ya = (u2f(t[0].x) + u2f(t[1].x)) + (u2f(t[2].x) + u2f(t[3].x)), yb = (u2f(t[0].z) + u2f(t[1].z)) + (u2f(t[2].z) + u2f(t[3].z));
      ya += dpp<XOR8>(ya); yb += dpp<XOR8>(yb);
      ya = xrow_sum(ya); yb = xrow_sum(yb);
      x1 = xr2 + rbf2(ga * rbf2(f32x2{ya, yb}));
      const unsigned xp = pack2c(x1.x, x1.y), xn = dppu<XOR1>(xp);
      if ((lane & 1) == 0) stsc(rws, a_xg, u32x4{xp, tag, xn, tag});
      x1 = unp2(xp);
      MISC[32] = 1u;
    }
    if (w < 6) {
      lds_wait(MISC + 32, 1u);
      __builtin_amdgcn_s_sleep(H4_DELAY);
      u32x4 t[2];
      const int po = WS_PG + (rb * 40 + rt) * 12288 + tid * 16;
      const int off[2] = {po, po + 6144};
      sweep<2, 1>(rws, off, t, tag);
      float ya = u2f(t[0].x) + u2f(t[1].x), yb = u2f(t[0].z) + u2f(t[1].z);
      ya += dpp<XOR8>(ya); yb += dpp<XOR8>(yb);
      ya = xrow_sum(ya); yb = xrow_sum(yb);
      if (lane < 8) *(LAS u32x2*)(L + L_RED + w * 64 + lane * 8) = u32x2{f2u(ya), f2u(yb)};
    }
    wg_barrier();
    if (tid < 8) {
      f32x2 r[6];
#pragma unroll
      for (int k = 0; k < 6; ++k) r[k] = *(LAS f32x2*)(L + L_RED + k * 64 + lane * 8);
      const f32x2 o = x1 + rbf2(unp2(gb2) * rbf2(((r[0] + r[1]) + (r[2] + r[3])) + (r[4] + r[5])));
      ((unsigned*)out)[rb * 320 + 8 * rt + lane] = pack2(o.x, o.y);
      if (bid == 0 && tid == 0) __hip_atomic_store((GAS unsigned*)(ws + WS_EPOCH), epoch + 1u, __ATOMIC_RELAXED, __HIP_MEMORY_SCOPE_AGENT);
    }
  } else {
    u32x4 BG[5]; u32x2 BD[5];
    {
      u32x4 SO[7];
      const rsrc_t rg = mkrs(w < 4 ? wg : wu, D * FF * 2), rd = mkrs(wd + (size_t)mid * 16 * D, 16 * D * 2);
#pragma unroll
      for (int k = 0; k < 4; ++k) {
        const int r = ((tid & 255) >> 1) + 128 * k, row = r < 160 * (mid & 3) ? r : r + 160;
        SO[k] = ldx<0>(rg, row * 3072 + mid * 32 + (tid & 1) * 16, 0);
      }
#pragma unroll
      for (int k = 0; k < 3; ++k) SO[4 + k] = ldnt(rd, tid * 16 + k * 8192, 0);
#pragma unroll
      for (int k = 0; k < 4; ++k) {
        const int r = ((tid & 255) >> 1) + 128 * k, row = r < 160 * (mid & 3) ? r : r + 160;
        if (r < 480) *(LAS u32x4*)(L + L_GU + row * 64 + (row >> 3) * 32 + (w >> 2) * 32 + (tid & 1) * 16) = SO[k];
      }
#pragma unroll
      for (int k = 0; k < 3; ++k) { const int u = tid + 512 * k; if (u < 1280) *(LAS u32x4*)(L + L_WD + (u / 80) * 1312 + (u % 80) * 16) = SO[4 + k]; }
      wg_barrier();
    }
    {
      LAS unsigned char* pb = L + L_GU + (w >> 1) * (5 * 2176) + g * 544 + trq * 64 + trp * 8 + (w & 1) * 32;
#pragma unroll
      for (int ks = 0; ks < 5; ++ks) BG[ks] = trfrag(pb + ks * 2176, 256);
      LAS unsigned char* pd = L + L_WD + (4 * g + trq) * 1312 + trp * 8 + w * 160;
#pragma unroll
      for (int t = 0; t < 5; ++t) BD[t] = trread(pd + t * 32);
    }
    int a_pg = WS_PG + ((g * 40 + 5 * w + (c & 1)) * 96 + mid) * 128 + (c & 14) * 8;
    PIN(a_pg);
    if (w < 4) {
      const int u2 = 128 + (lane & 31);
      f32x2 sb[6], cb[6];
      {
        u32x4 t[6];
        const int mo = WS_MG + w * 15360 + 960 * 8;
        const int off[6] = {mo + lane * 16, mo + 1024 + lane * 16, mo + u2 * 16, mo + 2560 + lane * 16, mo + 3584 + lane * 16, mo + 2560 + u2 * 16};
        sweep<6, 2>(rws, off, t, tag);
#pragma unroll
        for (int k = 0; k < 3; ++k) {
          sb[2 * k] = unp2(t[k].x); sb[2 * k + 1] = unp2(t[k].z);
          cb[2 * k] = rbf2(1.f + unp2(t[3 + k].x)); cb[2 * k + 1] = rbf2(1.f + unp2(t[3 + k].z));
        }
#pragma unroll
        for (int k = 0; k < 6; ++k) { PIN(sb[k]); PIN(cb[k]); }
      }
      int a_w0 = L_A + w * 1344 + lane * 8, a_w2 = L_A + w * 1344 + u2 * 8;
      float wq2 = lane < 32 ? 1.f : 0.f;
      PIN(a_w0); PIN(a_w2); PIN(wq2);
      u32x4 t[3];
      const int xo = WS_XG + x * 10240 + w * 2560;
      const int off[3] = {xo + lane * 16, xo + 1024 + lane * 16, xo + u2 * 16};
      sweep<3, 1, true>(rws, off, t, tag, (lane & 31) < 4);
      f32x2 xv[6];
#pragma unroll
      for (int k = 0; k < 3; ++k) { xv[2 * k] = unp2(t[k].x); xv[2 * k + 1] = unp2(t[k].z); }
      const f32x2 s2 = ((xv[0] + xv[1]) + (xv[2] + xv[3])) + wq2 * (xv[4] + xv[5]);
      const float mean = wave_sum(s2.x + s2.y) * (1.f / D);
      f32x2 va = {0.f, 0.f}, vb = {0.f, 0.f};
#pragma unroll
      for (int k = 0; k < 6; ++k) { const f32x2 dlt = xv[k] - mean; if (k >= 4) vb = __builtin_elementwise_fma(dlt, dlt, vb); else va = __builtin_elementwise_fma(dlt, dlt, va); }
      va += wq2 * vb;
      const float var = wave_sum(va.x + va.y) * (1.f / D);
      const float mb = rbf(mean), rd = __builtin_amdgcn_rcpf(ln_den(var));
      unsigned hp[6];
#pragma unroll
      for (int k = 0; k < 6; ++k) {
        const f32x2 hv = rbf2(rbf2(rbf2(xv[k] - mb) * rd) * cb[k]) + sb[k];
        hp[k] = pack2(hv.x, hv.y);
      }
      *(LAS u32x2*)(L + a_w0) = u32x2{hp[0], hp[1]};
      *(LAS u32x2*)(L + a_w0 + 512) = u32x2{hp[2], hp[3]};
      *(LAS u32x2*)(L + a_w2) = u32x2{hp[4], hp[5]};
    }
    wg_barrier();
    {
      u32x4 af[5];
      LAS unsigned char* pa = L + L_A + (c >> 2) * 1344 + (w >> 1) * 320 + g * 16;
#pragma unroll
      for (int ks = 0; ks < 5; ++ks) af[ks] = *(LAS u32x4*)(pa + ks * 64);
      f32x4 a0 = {0, 0, 0, 0}, a1 = {0, 0, 0, 0};
      a0 = mfma32(af[0], BG[0], a0); a1 = mfma32(af[1], BG[1], a1); a0 = mfma32(af[2], BG[2], a0); a1 = mfma32(af[3], BG[3], a1); a0 = mfma32(af[4], BG[4], a0);
      *(LAS float*)(L + L_RED + w * 256 + lane * 4) = a0[0] + a1[0];
    }
    wg_barrier();
    {
      f32x4 r[8];
#pragma unroll
      for (int k = 0; k < 8; ++k) r[k] = *(LAS f32x4*)(L + L_RED + k * 256 + (c >> 2) * 64 + g * 16);
      const f32x4 gs = (r[0] + r[2]) + (r[4] + r[6]), us = (r[1] + r[3]) + (r[5] + r[7]);
      const f32x2 g0 = rbf2(f32x2{gs[0], gs[1]}), g1 = rbf2(f32x2{gs[2], gs[3]}), p0 = rbf2(f32x2{us[0], us[1]}), p1 = rbf2(f32x2{us[2], us[3]});
      const float h0 = g0.x * (g0.x > 0.f ? 1.f : 0.f) * p0.x, h1 = g0.y * (g0.y > 0.f ? 1.f : 0.f) * p0.y;
      const float h2 = g1.x * (g1.x > 0.f ? 1.f : 0.f) * p1.x, h3 = g1.y * (g1.y > 0.f ? 1.f : 0.f) * p1.y;
      const u32x2 af = {pack2c(h0, h1), pack2c(h2, h3)};
      f32x4 y[5];
#pragma unroll
      for (int t = 0; t < 5; ++t)
        y[t] = __builtin_amdgcn_mfma_f32_16x16x16bf16_1k(__builtin_bit_cast(s16x4, af), __builtin_bit_cast(s16x4, BD[t]), f32x4{0, 0, 0, 0}, 0, 0, 0);
      const bool od = c & 1;
#pragma unroll
      for (int t = 0; t < 4; t += 2) {
        const float keep = od ? y[t + 1][0] : y[t][0], got = dpp<XOR1>(od ? y[t][0] : y[t + 1][0]);
        stsc(rws, a_pg + t * 12288, u32x4{f2u(od ? got : keep), tag, f2u(od ? keep : got), tag});
      }
      const float yn = dpp<XOR1>(y[4][0]);
      if (!od) stsc(rws, a_pg + 4 * 12288, u32x4{f2u(y[4][0]), tag, f2u(yn), tag});
    }
  }
}

extern "C" void kernel_launch(void* const* d_in, const int* in_sizes, int n_in, void* d_out, int out_size, void* d_ws, size_t ws_size, hipStream_t stream) {
  fused_block<<<256, 512, 0, stream>>>((const u16*)d_in[0], (const u16*)d_in[1], (const u16*)d_in[2], (const u16*)d_in[3], (const u16*)d_in[4],
                                       (const u16*)d_in[6], (const u16*)d_in[7], (const u16*)d_in[8], (const u16*)d_in[9], (const u16*)d_in[10],
                                       (u16*)d_out, (unsigned char*)d_ws);
}
```
